```python
import torch

def _softmax(x, axis=-1):
    return torch.softmax(x, dim=axis)
def _gelu(x):
    return torch.nn.functional.gelu(x, approximate="tanh")
def _silu(x):
    return torch.nn.functional.silu(x)
def _sigmoid(x):
    return torch.sigmoid(x)
def _layernorm(x, axis=-1, eps=1e-5):
    m = x.mean(dim=axis, keepdim=True)
    v = x.var(dim=axis, keepdim=True, unbiased=False)
    return (x - m) / torch.sqrt(v + eps)
def _scatter_add(tgt, idx, src, axis=0):
    return tgt.clone().index_add_(axis, idx, src)
def _segment_sum(src, seg, n):
    out = torch.zeros((n,) + src.shape[1:], dtype=src.dtype)
    return out.index_add_(0, seg, src)
def _pad(x, pw):
    flat = tuple(v for pair in reversed(pw) for v in pair)
    return torch.nn.functional.pad(x, flat)

kernel_name = 'concat_heads__gqa_block_cached__gqa_block_cached__near_ridge'

def setup_inputs():

    _w = (lambda a: (lambda t: t.to(torch.bfloat16) if t.is_floating_point() else t)(torch.as_tensor(a)))
    def _A_setup():
        B, Sq, D = 16, 32, 1024
        Hq, Hkv, Sk, Dh = 32, 4, 32, 128
        assert Hq % Hkv == 0, (Hq, Hkv)
        return {
            "x": _w((lambda *s: torch.randn(*s, dtype=torch.bfloat16) * 0.8)(B, Sq, D)),
            "k_cache": _w((lambda *s: torch.randn(*s, dtype=torch.bfloat16) * 0.8)(B, Hkv, Sk, Dh)),
            "v_cache": _w((lambda *s: torch.randn(*s, dtype=torch.bfloat16) * 0.05)(B, Hkv, Sk, Dh)),
            "wq": _w((lambda *s: torch.randn(*s, dtype=torch.bfloat16) * 0.8)(D, Hq * Dh) * (D ** -0.5)),
            "wo": _w((lambda *s: torch.randn(*s, dtype=torch.bfloat16) * 0.05)(Hq * Dh, D) * ((Hq * Dh) ** -0.5)),
        }
    _A_in = _A_setup()

    def _B_setup():
        B, Sq, D = 16, 32, 1024
        Hq, Hkv, Sk, Dh = 32, 4, 32, 128
        assert Hq % Hkv == 0, (Hq, Hkv)
        return {
            "x": _w((lambda *s: torch.randn(*s, dtype=torch.bfloat16) * 0.8)(B, Sq, D)),
            "k_cache": _w((lambda *s: torch.randn(*s, dtype=torch.bfloat16) * 0.8)(B, Hkv, Sk, Dh)),
            "v_cache": _w((lambda *s: torch.randn(*s, dtype=torch.bfloat16) * 0.05)(B, Hkv, Sk, Dh)),
            "wq": _w((lambda *s: torch.randn(*s, dtype=torch.bfloat16) * 0.8)(D, Hq * Dh) * (D ** -0.5)),
            "wo": _w((lambda *s: torch.randn(*s, dtype=torch.bfloat16) * 0.05)(Hq * Dh, D) * ((Hq * Dh) ** -0.5)),
        }
    _B_in = _B_setup()

    return {
        "A_x": _A_in["x"],
        "A_k_cache": _A_in["k_cache"],
        "A_v_cache": _A_in["v_cache"],
        "A_wq": _A_in["wq"],
        "A_wo": _A_in["wo"],
        "B_x": _B_in["x"],
        "B_k_cache": _B_in["k_cache"],
        "B_v_cache": _B_in["v_cache"],
        "B_wq": _B_in["wq"],
        "B_wo": _B_in["wo"],
    }

def reference(A_x, A_k_cache, A_v_cache, A_wq, A_wo, B_x, B_k_cache, B_v_cache, B_wq, B_wo):
    def _A_ref(x, k_cache, v_cache, wq, wo):
        B, Sq, D = x.shape
        Hq, Dh = 32, 128
        Hkv = k_cache.shape[1]
        rep = Hq // Hkv
        q = (lambda x, *d: x.permute(*d))((x @ wq).reshape(B, Sq, Hq, Dh), 0, 2, 1, 3)
        k = (lambda x, n, axis: torch.repeat_interleave(x, n, dim=axis))(k_cache, rep, axis=1)
        v = (lambda x, n, axis: torch.repeat_interleave(x, n, dim=axis))(v_cache, rep, axis=1)
        scores = torch.einsum("bhqd,bhkd->bhqk", q, k) * (Dh ** -0.5)
        p = _softmax(scores, axis=-1)
        o = torch.einsum("bhqk,bhkd->bhqd", p, v)
        return (lambda x, *d: x.permute(*d))(o, 0, 2, 1, 3).reshape(B, Sq, Hq * Dh) @ wo

    def _B_ref(x, k_cache, v_cache, wq, wo):
        B, Sq, D = x.shape
        Hq, Dh = 32, 128
        Hkv = k_cache.shape[1]
        rep = Hq // Hkv
        q = (lambda x, *d: x.permute(*d))((x @ wq).reshape(B, Sq, Hq, Dh), 0, 2, 1, 3)
        k = (lambda x, n, axis: torch.repeat_interleave(x, n, dim=axis))(k_cache, rep, axis=1)
        v = (lambda x, n, axis: torch.repeat_interleave(x, n, dim=axis))(v_cache, rep, axis=1)
        scores = torch.einsum("bhqd,bhkd->bhqk", q, k) * (Dh ** -0.5)
        p = _softmax(scores, axis=-1)
        o = torch.einsum("bhqk,bhkd->bhqd", p, v)
        return (lambda x, *d: x.permute(*d))(o, 0, 2, 1, 3).reshape(B, Sq, Hq * Dh) @ wo

    a = _A_ref(A_x, A_k_cache, A_v_cache, A_wq, A_wo)
    b = _B_ref(B_x, B_k_cache, B_v_cache, B_wq, B_wo)
    return torch.concatenate([a, b], axis=-1)
```

```cpp
#include <hip/hip_runtime.h>
#include <hip/hip_bf16.h>
typedef unsigned short u16;
__device__ __forceinline__ float b2f(u16 v){ return __uint_as_float(((unsigned)v)<<16);}
__device__ __forceinline__ u16 f2b(float f){ __hip_bfloat16 h=__float2bfloat16(f); return *(u16*)&h; }
__global__ void k_q(const u16* x,const u16* wq,u16* q){
  int n=blockIdx.x*256+threadIdx.x; int m=blockIdx.y; float a=0;
  for(int k=0;k<1024;++k) a+=b2f(x[m*1024+k])*b2f(wq[(size_t)k*4096+n]);
  q[(size_t)m*4096+n]=f2b(a);
}
__global__ void k_p(const u16* q,const u16* kc,u16* P){
  int b=blockIdx.y,h=blockIdx.x; int t=threadIdx.x; int qi=t>>5, ki=t&31;
  float s=0; for(int d=0;d<128;++d) s+=b2f(q[(size_t)(b*32+qi)*4096+h*128+d])*b2f(kc[((b*4+(h>>3))*32+ki)*128+d]);
  s*=0.08838834764831845f;
  float m=s; for(int o=16;o;o>>=1) m=fmaxf(m,__shfl_xor(m,o,32));
  float e=__expf(s-m); float l=e; for(int o=16;o;o>>=1) l+=__shfl_xor(l,o,32);
  P[((b*32+h)*32+qi)*32+ki]=f2b(e/l);
}
__global__ void k_vw(const u16* v,const u16* wo,u16* VW){
  int n=blockIdx.x*256+threadIdx.x; int r=blockIdx.y;
  int b=r>>10,h=(r>>5)&31,k=r&31; float a=0;
  for(int d=0;d<128;++d) a+=b2f(v[((b*4+(h>>3))*32+k)*128+d])*b2f(wo[(size_t)(h*128+d)*1024+n]);
  VW[(size_t)r*1024+n]=f2b(a);
}
__global__ void k_o(const u16* P,const u16* VW,u16* out,int off){
  int n=blockIdx.x*256+threadIdx.x; int m=blockIdx.y; int b=m>>5,qi=m&31; float a=0;
  for(int h=0;h<32;++h) for(int k=0;k<32;++k) a+=b2f(P[((b*32+h)*32+qi)*32+k])*b2f(VW[(size_t)((b*32+h)*32+k)*1024+n]);
  out[(size_t)m*2048+off+n]=f2b(a);
}
extern "C" void kernel_launch(void* const* d_in, const int* in_sizes, int n_in, void* d_out, int out_size, void* d_ws, size_t ws_size, hipStream_t stream){
  for(int br=0;br<2;++br){
    const u16* x=(const u16*)d_in[br*5+0]; const u16* kc=(const u16*)d_in[br*5+1]; const u16* vc=(const u16*)d_in[br*5+2];
    const u16* wq=(const u16*)d_in[br*5+3]; const u16* wo=(const u16*)d_in[br*5+4];
    u16* q=(u16*)d_ws; u16* P=q+512*4096; u16* VW=P+16*32*32*32;
    k_q<<<dim3(16,512),256,0,stream>>>(x,wq,q);
    k_p<<<dim3(32,16),1024,0,stream>>>(q,kc,P);
    k_vw<<<dim3(4,16384),256,0,stream>>>(vc,wo,VW);
    k_o<<<dim3(4,512),256,0,stream>>>(P,VW,(u16*)d_out,br*1024);
  }
}
```

```cpp
#include <hip/hip_runtime.h>
#include <hip/hip_bf16.h>
typedef unsigned short u16;
typedef __bf16 bf16x4 __attribute__((ext_vector_type(4)));
typedef __bf16 bf16x8 __attribute__((ext_vector_type(8)));
typedef float f32x16 __attribute__((ext_vector_type(16)));
typedef float f32x4 __attribute__((ext_vector_type(4)));
typedef unsigned u32x4 __attribute__((ext_vector_type(4)));
typedef unsigned u32x2 __attribute__((ext_vector_type(2)));
typedef int i32x4 __attribute__((ext_vector_type(4)));
#define LDS __attribute__((address_space(3)))
#define DI __device__ __forceinline__
#define MFMA(a,b,c) __builtin_amdgcn_mfma_f32_32x32x16_bf16((a),(b),(c),0,0,0)

DI i32x4 make_srd(const void* p, unsigned bytes){
  unsigned long long a=(unsigned long long)p;
  i32x4 r; r[0]=__builtin_amdgcn_readfirstlane((unsigned)a); r[1]=__builtin_amdgcn_readfirstlane((unsigned)(a>>32))&0xffff; r[2]=bytes; r[3]=0x00020000; return r;
}
DI void dma16(i32x4 srd, unsigned voff, unsigned soff, unsigned lds){
  asm volatile("s_mov_b32 m0, %3\n\ts_nop 0\n\tbuffer_load_dwordx4 %0, %1, %2 offen lds" :: "v"(voff), "s"(srd), "s"(soff), "s"(lds) : "memory");
}
template<int N> DI void waitv(){ asm volatile("s_waitcnt vmcnt(%0)"::"i"(N):"memory"); }
DI void bar(){ asm volatile("s_waitcnt lgkmcnt(0)\n\ts_barrier":::"memory"); }
DI bf16x8 ldsr128(unsigned a){ return *(const LDS bf16x8*)(size_t)a; }
DI bf16x8 ldstr(unsigned a0, unsigned a1){
  bf16x4 lo=__builtin_amdgcn_ds_read_tr16_b64_v4bf16((LDS bf16x4*)(size_t)a0);
  bf16x4 hi=__builtin_amdgcn_ds_read_tr16_b64_v4bf16((LDS bf16x4*)(size_t)a1);
  return __builtin_shufflevector(lo,hi,0,1,2,3,4,5,6,7);
}
DI bf16x8 pack8(const f32x16& x, int s){
  bf16x8 r;
  #pragma unroll
  for(int j=0;j<8;++j) r[j]=(__bf16)x[8*s+j];
  return r;
}
DI float xhalf_max(float v){ auto r=__builtin_amdgcn_permlane32_swap(__float_as_uint(v),__float_as_uint(v),false,false); return fmaxf(__uint_as_float(r[0]),__uint_as_float(r[1])); }
DI float xhalf_sum(float v){ auto r=__builtin_amdgcn_permlane32_swap(__float_as_uint(v),__float_as_uint(v),false,false); return __uint_as_float(r[0])+__uint_as_float(r[1]); }

constexpr int NS1=3;
constexpr unsigned L1_K=NS1*32768, L1_X=L1_K+32768, L1_END=L1_X+16384;

__global__ __launch_bounds__(512) void k_ph1(const u16* xA,const u16* kA,const u16* wA,const u16* xB,const u16* kB,const u16* wB,u16* P,const char* oA,const char* oB){
  extern __shared__ __attribute__((aligned(16))) char smem[];
  const unsigned lds0=(unsigned)(size_t)(LDS char*)smem;
  const int tid=threadIdx.x, l=tid&63; const int wid=__builtin_amdgcn_readfirstlane(tid>>6);
  const int bid=blockIdx.x, xcd=bid&7, j32=bid>>3, br=xcd>>2, g=xcd&3, bg=j32>>3, j=j32&7, head=8*g+j;

  if(wid>=4){
    const int lw=wid-4;
    i32x4 sx=make_srd(br?xB:xA,512*2048), sw=make_srd(br?wB:wA,1024*8192), sk=make_srd(br?kB:kA,16*4*32*256);
    unsigned vx[4],vw[4];
    #pragma unroll
    for(int i=0;i<4;++i){ int p=4*lw+i; int row=8*p+(l>>3); vx[i]=(128*bg+row)*2048+(((l&7)^((row>>1)&7))<<4);
      int kr=4*p+(l>>4); vw[i]=kr*8192+head*256+(((l&15)^((l>>4)<<2))<<4); }
    auto issue=[&](int t,int st){
      #pragma unroll
      for(int i=0;i<4;++i) dma16(sx,vx[i],t*128,lds0+st*32768+(4*lw+i)*1024);
      #pragma unroll
      for(int i=0;i<4;++i) dma16(sw,vw[i],t*64*8192,lds0+st*32768+16384+(4*lw+i)*1024);
    };
    issue(0,0); issue(1,1);
    int st=2;
    for(int t=0;t<14;++t){ waitv<8>(); bar(); issue(t+2,st); st=(st==2)?0:st+1; }
    waitv<8>(); bar();
    #pragma unroll
    for(int i=0;i<8;++i){ int p=8*lw+i; int key=4*(p&7)+(l>>4); dma16(sk,(((4*bg+lw)*4+g)*32+key)*256+(((l&15)^(key&15))<<4),0,lds0+L1_K+p*1024); }
    waitv<8>(); bar();
    waitv<0>(); bar();
    bar();
  } else {
    const int wd=wid&1, wq=wid>>1, r=l&31, h=l>>5, blk=(l>>4)&1, q=(l&15)>>2, p=l&3;
    unsigned aw[2], ax[4];
    #pragma unroll
    for(int i=0;i<2;++i){ int db=2*wd+i; aw[i]=lds0+16384+256*(8*h+q)+16*(((db^q)<<2)|(2*blk+(p&1)))+8*(p>>1); }
    #pragma unroll
    for(int s=0;s<4;++s) ax[s]=lds0+128*(64*wq+r)+16*((2*s+h)^((r>>1)&7));
    f32x16 acc[2][2]={};
    bf16x8 fa[2],fb[2];
    unsigned so=0;
    bar();
    fa[0]=ldstr(aw[0],aw[0]+1024); fa[1]=ldstr(aw[1],aw[1]+1024); fb[0]=ldsr128(ax[0]); fb[1]=ldsr128(ax[0]+4096);
    for(int t=0;t<16;++t){
      #pragma unroll
      for(int s=0;s<4;++s){
        bf16x8 na[2],nb[2];
        if(s<3){
          na[0]=ldstr(aw[0]+so+4096*(s+1),aw[0]+so+4096*(s+1)+1024); na[1]=ldstr(aw[1]+so+4096*(s+1),aw[1]+so+4096*(s+1)+1024);
          nb[0]=ldsr128(ax[s+1]+so); nb[1]=ldsr128(ax[s+1]+so+4096);
        } else {
          bar();
          so=(so==2*32768)?0:so+32768;
          na[0]=ldstr(aw[0]+so,aw[0]+so+1024); na[1]=ldstr(aw[1]+so,aw[1]+so+1024);
          nb[0]=ldsr128(ax[0]+so); nb[1]=ldsr128(ax[0]+so+4096);
        }
        acc[0][0]=MFMA(fa[0],fb[0],acc[0][0]); acc[0][1]=MFMA(fa[0],fb[1],acc[0][1]);
        acc[1][0]=MFMA(fa[1],fb[0],acc[1][0]); acc[1][1]=MFMA(fa[1],fb[1],acc[1][1]);
        fa[0]=na[0];fa[1]=na[1];fb[0]=nb[0];fb[1]=nb[1];
      }
    }
    f32x16 S[2]={};
    #pragma unroll
    for(int qi=0;qi<2;++qi)
      #pragma unroll
      for(int di=0;di<2;++di)
        #pragma unroll
        for(int s=0;s<2;++s){
          bf16x8 kf=ldsr128(lds0+L1_K+(2*wq+qi)*8192+256*r+16*((4*(2*wd+di)+2*s+h)^(r&15)));
          S[qi]=MFMA(kf,pack8(acc[di][qi],s),S[qi]);
        }
    f32x16 own = wd? S[1]:S[0], oth = wd? S[0]:S[1];
    #pragma unroll
    for(int i=0;i<4;++i){ f32x4 v={oth[4*i],oth[4*i+1],oth[4*i+2],oth[4*i+3]}; *(LDS f32x4*)(size_t)(lds0+L1_X+wid*4096+i*1024+l*16)=v; }
    bar();
    #pragma unroll
    for(int i=0;i<4;++i){ f32x4 v=*(const LDS f32x4*)(size_t)(lds0+L1_X+(wid^1)*4096+i*1024+l*16); own[4*i]+=v[0];own[4*i+1]+=v[1];own[4*i+2]+=v[2];own[4*i+3]+=v[3]; }
    float m=own[0];
    #pragma unroll
    for(int i=1;i<16;++i) m=fmaxf(m,own[i]);
    m=xhalf_max(m);
    const float c=0.08838834764831845f*1.4426950408889634f;
    float mc=m*c, sum=0;
    #pragma unroll
    for(int i=0;i<16;++i){ own[i]=__builtin_amdgcn_exp2f(own[i]*c-mc); sum+=own[i]; }
    sum=xhalf_sum(sum);
    float inv=__builtin_amdgcn_rcpf(sum);
    #pragma unroll
    for(int i=0;i<16;++i) own[i]*=inv;
    const int b=4*bg+2*wq+wd;
    char* pp=(char*)P+((((br*8+(b>>1))*32+head)*2+(b&1))*2)*1024+l*16;
    *(bf16x8*)pp=pack8(own,0); *(bf16x8*)(pp+1024)=pack8(own,1);
  }
}

constexpr int NS2=4;
constexpr unsigned L2_V=NS2*20480, L2_END=L2_V+32768;
__global__ __launch_bounds__(512) void k_ph2(const u16* vA,const u16* wA,const u16* vB,const u16* wB,const u16* P,u16* out){
  extern __shared__ __attribute__((aligned(16))) char smem[];
  const unsigned lds0=(unsigned)(size_t)(LDS char*)smem;
  const int tid=threadIdx.x, l=tid&63; const int wid=__builtin_amdgcn_readfirstlane(tid>>6);
  const int bid=blockIdx.x, xcd=bid&7, j32=bid>>3, br=xcd>>2, xq=xcd&3, bg=j32>>3, j=j32&7;
  const int bp=2*bg+(j>>2), col0=256*xq+64*(j&3);
  if(wid>=4){
    const int lw=wid-4;
    i32x4 sw=make_srd(br?wB:wA,4096*2048), sv=make_srd(br?vB:vA,16*4*32*256), sp=make_srd(P,2<<20);
    unsigned vw[4],vv[4];
    #pragma unroll
    for(int i=0;i<4;++i){ int p=4*lw+i; int row=8*p+(l>>3); vw[i]=row*2048+col0*2+(((l&7)^(((row>>1)&1)<<2))<<4);
      int key=4*(p&7)+(l>>4); vv[i]=(((2*bp+(p>>3))*4)*32+key)*256+(((l&15)^(key&15))<<4); }
    const unsigned vp=(br*8+bp)*32*4096+lw*1024+l*16;
    auto issue=[&](int hh,int st){
      #pragma unroll
      for(int i=0;i<4;++i) dma16(sw,vw[i],hh*128*2048,lds0+st*20480+(4*lw+i)*1024);
      dma16(sp,vp,hh*4096,lds0+st*20480+16384+lw*1024);
    };
    auto issueV=[&](int g){
      #pragma unroll
      for(int i=0;i<4;++i) dma16(sv,vv[i],g*8192,lds0+L2_V+(g&1)*16384+(4*lw+i)*1024);
    };
    issueV(0); issue(0,0); issue(1,1); issue(2,2);
    for(int t=0;t<29;++t){
      if(t<24 && (t&7)>=2 && (t&7)<=4){ waitv<14>(); } else waitv<10>();
      bar(); issue(t+3,(t+3)&3);
      if((t&7)==1 && t<24) issueV((t>>3)+1);
    }
    waitv<0>(); bar(); bar(); bar();
  } else {
    const int bl=wid>>1, ch=wid&1, r=l&31, h=l>>5, blk=(l>>4)&1, q=(l&15)>>2, p=l&3;
    const unsigned aw=lds0+128*(8*h+q)+16*(((ch^(q>>1))<<2)|(2*blk+(p>>1)))+8*(p&1);
    const unsigned ap=lds0+16384+bl*2048+l*16;
    f32x16 o={};
    bf16x8 vf[8];
    for(int t=0;t<32;++t){
      bar();
      const unsigned so=(t&3)*20480;
      if((t&7)==0){
        #pragma unroll
        for(int s=0;s<8;++s) vf[s]=ldsr128(lds0+L2_V+((t>>3)&1)*16384+bl*8192+256*r+16*((2*s+h)^(r&15)));
      }
      f32x16 X={};
      #pragma unroll
      for(int s=0;s<8;++s){ bf16x8 wf=ldstr(aw+so+2048*s,aw+so+2048*s+512); X=MFMA(vf[s],wf,X); }
      bf16x8 p0=ldsr128(ap+so), p1=ldsr128(ap+so+1024);
      o=MFMA(pack8(X,0),p0,o); o=MFMA(pack8(X,1),p1,o);
    }
    char* op=(char*)out+((size_t)((2*bp+bl)*32+r)*2048+br*1024+col0+32*ch+4*h)*2;
    #pragma unroll
    for(int i=0;i<4;++i){ bf16x4 v={(__bf16)o[4*i],(__bf16)o[4*i+1],(__bf16)o[4*i+2],(__bf16)o[4*i+3]}; *(bf16x4*)(op+16*i)=v; }
  }
}

extern "C" void kernel_launch(void* const* d_in, const int* in_sizes, int n_in, void* d_out, int out_size, void* d_ws, size_t ws_size, hipStream_t stream){
  (void)hipFuncSetAttribute((const void*)k_ph1,hipFuncAttributeMaxDynamicSharedMemorySize,L1_END);
  (void)hipFuncSetAttribute((const void*)k_ph2,hipFuncAttributeMaxDynamicSharedMemorySize,L2_END);
  const u16* const* in=(const u16* const*)d_in;
  k_ph1<<<256,512,L1_END,stream>>>(in[0],in[1],in[3],in[5],in[6],in[8],(u16*)d_ws,(const char*)in[4],(const char*)in[9]);
  k_ph2<<<256,512,L2_END,stream>>>(in[2],in[4],in[7],in[9],(const u16*)d_ws,(u16*)d_out);
}
```

```cpp
#include <hip/hip_runtime.h>
#include <hip/hip_bf16.h>
typedef unsigned short u16;
typedef __bf16 bf16x4 __attribute__((ext_vector_type(4)));
typedef __bf16 bf16x8 __attribute__((ext_vector_type(8)));
typedef float f32x16 __attribute__((ext_vector_type(16)));
typedef float f32x4 __attribute__((ext_vector_type(4)));
typedef unsigned u32x4 __attribute__((ext_vector_type(4)));
typedef unsigned u32x2 __attribute__((ext_vector_type(2)));
typedef int i32x4 __attribute__((ext_vector_type(4)));
#define LDS __attribute__((address_space(3)))
#define DI __device__ __forceinline__
#define MFMA(a,b,c) __builtin_amdgcn_mfma_f32_32x32x16_bf16((a),(b),(c),0,0,0)

DI i32x4 make_srd(const void* p, unsigned bytes){
  unsigned long long a=(unsigned long long)p;
  i32x4 r; r[0]=__builtin_amdgcn_readfirstlane((unsigned)a); r[1]=__builtin_amdgcn_readfirstlane((unsigned)(a>>32))&0xffff; r[2]=bytes; r[3]=0x00020000; return r;
}
DI void dma16(i32x4 srd, unsigned voff, unsigned soff, unsigned lds){
  asm volatile("s_mov_b32 m0, %3\n\ts_nop 0\n\tbuffer_load_dwordx4 %0, %1, %2 offen lds" :: "v"(voff), "s"(srd), "s"(soff), "s"(lds) : "memory");
}
template<int N> DI void waitv(){ asm volatile("s_waitcnt vmcnt(%0)"::"i"(N):"memory"); }
DI void bar(){ asm volatile("s_waitcnt lgkmcnt(0)\n\ts_barrier":::"memory"); }
DI bf16x8 ldsr128(unsigned a){ return *(const LDS bf16x8*)(size_t)a; }
DI bf16x8 ldstr(unsigned a0, unsigned a1){
  bf16x4 lo=__builtin_amdgcn_ds_read_tr16_b64_v4bf16((LDS bf16x4*)(size_t)a0);
  bf16x4 hi=__builtin_amdgcn_ds_read_tr16_b64_v4bf16((LDS bf16x4*)(size_t)a1);
  return __builtin_shufflevector(lo,hi,0,1,2,3,4,5,6,7);
}
DI bf16x8 pack8(const f32x16& x, int s){
  bf16x8 r;
  #pragma unroll
  for(int j=0;j<8;++j) r[j]=(__bf16)x[8*s+j];
  return r;
}
DI float xhalf_max(float v){ auto r=__builtin_amdgcn_permlane32_swap(__float_as_uint(v),__float_as_uint(v),false,false); return fmaxf(__uint_as_float(r[0]),__uint_as_float(r[1])); }
DI float xhalf_sum(float v){ auto r=__builtin_amdgcn_permlane32_swap(__float_as_uint(v),__float_as_uint(v),false,false); return __uint_as_float(r[0])+__uint_as_float(r[1]); }

constexpr int NS1=3;
constexpr unsigned L1_K=NS1*32768, L1_X=L1_K+32768, L1_END=L1_X+16384;

struct Big { unsigned w[9*32]; };
__global__ __launch_bounds__(512) void k_ph1(Big big,const u16* xA,const u16* kA,const u16* wA,const u16* xB,const u16* kB,const u16* wB,u16* P){
  extern __shared__ __attribute__((aligned(16))) char smem[];
  const unsigned lds0=(unsigned)(size_t)(LDS char*)smem;
  const int tid=threadIdx.x, l=tid&63; const int wid=__builtin_amdgcn_readfirstlane(tid>>6);
  const int bid=blockIdx.x, xcd=bid&7, j32=bid>>3, br=xcd>>2, g=xcd&3, bg=j32>>3, j=j32&7, head=8*g+j;
  { const char* ka=(const char*)__builtin_amdgcn_kernarg_segment_ptr(); unsigned off=128;
    for(int i=0;i<0;++i){ asm volatile("s_load_dword %0, %1, %0\n\ts_waitcnt lgkmcnt(0)" : "+s"(off) : "s"(ka) : "memory"); } }
  if(wid>=4){
    const int lw=wid-4;
    i32x4 sx=make_srd(br?xB:xA,512*2048), sw=make_srd(br?wB:wA,1024*8192), sk=make_srd(br?kB:kA,16*4*32*256);
    unsigned vx[4],vw[4];
    #pragma unroll
    for(int i=0;i<4;++i){ int p=4*lw+i; int row=8*p+(l>>3); vx[i]=(128*bg+row)*2048+(((l&7)^((row>>1)&7))<<4);
      int kr=4*p+(l>>4); vw[i]=kr*8192+head*256+(((l&15)^((l>>4)<<2))<<4); }
    auto issue=[&](int t,int st){
      #pragma unroll
      for(int i=0;i<4;++i) dma16(sx,vx[i],t*128,lds0+st*32768+(4*lw+i)*1024);
      #pragma unroll
      for(int i=0;i<4;++i) dma16(sw,vw[i],t*64*8192,lds0+st*32768+16384+(4*lw+i)*1024);
    };
    issue(0,0); issue(1,1);
    int st=2;
    for(int t=0;t<14;++t){ waitv<8>(); bar(); issue(t+2,st); st=(st==2)?0:st+1; }
    waitv<8>(); bar();
    #pragma unroll
    for(int i=0;i<8;++i){ int p=8*lw+i; int key=4*(p&7)+(l>>4); dma16(sk,(((4*bg+lw)*4+g)*32+key)*256+(((l&15)^(key&15))<<4),0,lds0+L1_K+p*1024); }
    waitv<8>(); bar();
    waitv<0>(); bar();
    bar();
  } else {
    const int wd=wid&1, wq=wid>>1, r=l&31, h=l>>5, blk=(l>>4)&1, q=(l&15)>>2, p=l&3;
    unsigned aw[2], ax[4];
    #pragma unroll
    for(int i=0;i<2;++i){ int db=2*wd+i; aw[i]=lds0+16384+256*(8*h+q)+16*(((db^q)<<2)|(2*blk+(p&1)))+8*(p>>1); }
    #pragma unroll
    for(int s=0;s<4;++s) ax[s]=lds0+128*(64*wq+r)+16*((2*s+h)^((r>>1)&7));
    f32x16 acc[2][2]={};
    bf16x8 fa[2],fb[2];
    unsigned so=0;
    bar();
    fa[0]=ldstr(aw[0],aw[0]+1024); fa[1]=ldstr(aw[1],aw[1]+1024); fb[0]=ldsr128(ax[0]); fb[1]=ldsr128(ax[0]+4096);
    for(int t=0;t<16;++t){
      #pragma unroll
      for(int s=0;s<4;++s){
        bf16x8 na[2],nb[2];
        if(s<3){
          na[0]=ldstr(aw[0]+so+4096*(s+1),aw[0]+so+4096*(s+1)+1024); na[1]=ldstr(aw[1]+so+4096*(s+1),aw[1]+so+4096*(s+1)+1024);
          nb[0]=ldsr128(ax[s+1]+so); nb[1]=ldsr128(ax[s+1]+so+4096);
        } else {
          bar();
          so=(so==2*32768)?0:so+32768;
          na[0]=ldstr(aw[0]+so,aw[0]+so+1024); na[1]=ldstr(aw[1]+so,aw[1]+so+1024);
          nb[0]=ldsr128(ax[0]+so); nb[1]=ldsr128(ax[0]+so+4096);
        }
        acc[0][0]=MFMA(fa[0],fb[0],acc[0][0]); acc[0][1]=MFMA(fa[0],fb[1],acc[0][1]);
        acc[1][0]=MFMA(fa[1],fb[0],acc[1][0]); acc[1][1]=MFMA(fa[1],fb[1],acc[1][1]);
        fa[0]=na[0];fa[1]=na[1];fb[0]=nb[0];fb[1]=nb[1];
      }
    }
    f32x16 S[2]={};
    #pragma unroll
    for(int qi=0;qi<2;++qi)
      #pragma unroll
      for(int di=0;di<2;++di)
        #pragma unroll
        for(int s=0;s<2;++s){
          bf16x8 kf=ldsr128(lds0+L1_K+(2*wq+qi)*8192+256*r+16*((4*(2*wd+di)+2*s+h)^(r&15)));
          S[qi]=MFMA(kf,pack8(acc[di][qi],s),S[qi]);
        }
    f32x16 own = wd? S[1]:S[0], oth = wd? S[0]:S[1];
    #pragma unroll
    for(int i=0;i<4;++i){ f32x4 v={oth[4*i],oth[4*i+1],oth[4*i+2],oth[4*i+3]}; *(LDS f32x4*)(size_t)(lds0+L1_X+wid*4096+i*1024+l*16)=v; }
    bar();
    #pragma unroll
    for(int i=0;i<4;++i){ f32x4 v=*(const LDS f32x4*)(size_t)(lds0+L1_X+(wid^1)*4096+i*1024+l*16); own[4*i]+=v[0];own[4*i+1]+=v[1];own[4*i+2]+=v[2];own[4*i+3]+=v[3]; }
    float m=own[0];
    #pragma unroll
    for(int i=1;i<16;++i) m=fmaxf(m,own[i]);
    m=xhalf_max(m);
    const float c=0.08838834764831845f*1.4426950408889634f;
    float mc=m*c, sum=0;
    #pragma unroll
    for(int i=0;i<16;++i){ own[i]=__builtin_amdgcn_exp2f(own[i]*c-mc); sum+=own[i]; }
    sum=xhalf_sum(sum);
    float inv=__builtin_amdgcn_rcpf(sum);
    #pragma unroll
    for(int i=0;i<16;++i) own[i]*=inv;
    const int b=4*bg+2*wq+wd;
    char* pp=(char*)P+((((br*8+(b>>1))*32+head)*2+(b&1))*2)*1024+l*16;
    *(bf16x8*)pp=pack8(own,0); *(bf16x8*)(pp+1024)=pack8(own,1);
  }
}

constexpr int NS2=4;
constexpr unsigned L2_V=NS2*20480, L2_END=L2_V+32768;
__global__ __launch_bounds__(512) void k_ph2(const u16* vA,const u16* wA,const u16* vB,const u16* wB,const u16* P,u16* out){
  extern __shared__ __attribute__((aligned(16))) char smem[];
  const unsigned lds0=(unsigned)(size_t)(LDS char*)smem;
  const int tid=threadIdx.x, l=tid&63; const int wid=__builtin_amdgcn_readfirstlane(tid>>6);
  const int bid=blockIdx.x, xcd=bid&7, j32=bid>>3, br=xcd>>2, xq=xcd&3, bg=j32>>3, j=j32&7;
  const int bp=2*bg+(j>>2), col0=256*xq+64*(j&3);
  if(wid>=4){
    const int lw=wid-4;
    i32x4 sw=make_srd(br?wB:wA,4096*2048), sv=make_srd(br?vB:vA,16*4*32*256), sp=make_srd(P,2<<20);
    unsigned vw[4],vv[4];
    #pragma unroll
    for(int i=0;i<4;++i){ int p=4*lw+i; int row=8*p+(l>>3); vw[i]=row*2048+col0*2+(((l&7)^(((row>>1)&1)<<2))<<4);
      int key=4*(p&7)+(l>>4); vv[i]=(((2*bp+(p>>3))*4)*32+key)*256+(((l&15)^(key&15))<<4); }
    const unsigned vp=(br*8+bp)*32*4096+lw*1024+l*16;
    auto issue=[&](int hh,int st){
      #pragma unroll
      for(int i=0;i<4;++i) dma16(sw,vw[i],hh*128*2048,lds0+st*20480+(4*lw+i)*1024);
      dma16(sp,vp,hh*4096,lds0+st*20480+16384+lw*1024);
    };
    auto issueV=[&](int g){
      #pragma unroll
      for(int i=0;i<4;++i) dma16(sv,vv[i],g*8192,lds0+L2_V+(g&1)*16384+(4*lw+i)*1024);
    };
    issueV(0); issue(0,0); issue(1,1); issue(2,2);
    for(int t=0;t<29;++t){
      if(t<24 && (t&7)>=2 && (t&7)<=4){ waitv<14>(); } else waitv<10>();
      bar(); issue(t+3,(t+3)&3);
      if((t&7)==1 && t<24) issueV((t>>3)+1);
    }
    waitv<0>(); bar(); bar(); bar();
  } else {
    const int bl=wid>>1, ch=wid&1, r=l&31, h=l>>5, blk=(l>>4)&1, q=(l&15)>>2, p=l&3;
    const unsigned aw=lds0+128*(8*h+q)+16*(((ch^(q>>1))<<2)|(2*blk+(p>>1)))+8*(p&1);
    const unsigned ap=lds0+16384+bl*2048+l*16;
    f32x16 o={};
    bf16x8 vf[8];
    for(int t=0;t<32;++t){
      bar();
      const unsigned so=(t&3)*20480;
      if((t&7)==0){
        #pragma unroll
        for(int s=0;s<8;++s) vf[s]=ldsr128(lds0+L2_V+((t>>3)&1)*16384+bl*8192+256*r+16*((2*s+h)^(r&15)));
      }
      f32x16 X={};
      #pragma unroll
      for(int s=0;s<8;++s){ bf16x8 wf=ldstr(aw+so+2048*s,aw+so+2048*s+512); X=MFMA(vf[s],wf,X); }
      bf16x8 p0=ldsr128(ap+so), p1=ldsr128(ap+so+1024);
      o=MFMA(pack8(X,0),p0,o); o=MFMA(pack8(X,1),p1,o);
    }
    char* op=(char*)out+((size_t)((2*bp+bl)*32+r)*2048+br*1024+col0+32*ch+4*h)*2;
    #pragma unroll
    for(int i=0;i<4;++i){ bf16x4 v={(__bf16)o[4*i],(__bf16)o[4*i+1],(__bf16)o[4*i+2],(__bf16)o[4*i+3]}; *(bf16x4*)(op+16*i)=v; }
  }
}

extern "C" void kernel_launch(void* const* d_in, const int* in_sizes, int n_in, void* d_out, int out_size, void* d_ws, size_t ws_size, hipStream_t stream){
  (void)hipFuncSetAttribute((const void*)k_ph1,hipFuncAttributeMaxDynamicSharedMemorySize,L1_END);
  (void)hipFuncSetAttribute((const void*)k_ph2,hipFuncAttributeMaxDynamicSharedMemorySize,L2_END);
  const u16* const* in=(const u16* const*)d_in;
  Big big; for(int i=0;i<9*32;++i) big.w[i]=((i/32)+1)*128;
  k_ph1<<<256,512,L1_END,stream>>>(big,in[0],in[1],in[3],in[5],in[6],in[8],(u16*)d_ws);
  k_ph2<<<256,512,L2_END,stream>>>(in[2],in[4],in[7],in[9],(const u16*)d_ws,(u16*)d_out);
}
```

```cpp
#include <hip/hip_runtime.h>
typedef int i32x4 __attribute__((ext_vector_type(4)));
typedef int i32x8 __attribute__((ext_vector_type(8)));
#define DI __device__ __forceinline__
constexpr unsigned WS_FLAG = 2u << 20, WS_XFLAG = WS_FLAG + 8192, WS_EPOCH = WS_FLAG + 16384, WS_STAMP = 3u << 20, WS_BIGF = WS_FLAG + 65536, WS_BIGX = WS_FLAG + 262144, WS_PART = 4u << 20;
typedef unsigned long long u64x2 __attribute__((ext_vector_type(2)));
DI u64x2 join(unsigned long long a, unsigned long long b) { u64x2 r; r[0] = a; r[1] = b; return r; }
typedef int i32x2 __attribute__((ext_vector_type(2)));
DI i32x2 pair(int a, int b) { i32x2 r; r[0] = a; r[1] = b; return r; }
DI int lane_id() { int l; asm volatile("v_mbcnt_lo_u32_b32 %0, -1, 0\n\tv_mbcnt_hi_u32_b32 %0, -1, %0" : "=v"(l)); return l; }
struct Args { const void* in[10]; void* out; void* ws; };
__global__ __launch_bounds__(512, 2) void gqa_fused(Args a) {

  unsigned long long px, pk, pv, pq, po, pout, pws, dx, dq, sxw0;
  unsigned tid = threadIdx.x, bidu = blockIdx.x, vx0, vx1, vx2, vx3, vw0, vw1, vw2, vw3, sl, st0;
  asm volatile(
    "98:\n"
    "s_bfe_u32 s44, s2, 0x10002\n"
    "s_mul_i32 s44, s44, 40\n"
    "s_load_dwordx2 s[8:9], s[0:1], s44\n"
    "s_load_dwordx2 s[12:13], s[0:1], s44 offset:24\n"
    "v_readfirstlane_b32 s38, v0\n"
    "v_and_b32 v1, 63, v0\n"
    "v_lshrrev_b32 v2, 4, v1\n"
    "v_and_b32 v3, 7, v1\n"
    "v_xor_b32 v3, v3, v2\n"
    "s_addk_i32 s38, -256\n"
    "v_lshrrev_b32 v4, 3, v1\n"
    "v_lshlrev_b32 v4, 11, v4\n"
    "s_lshl_b32 s38, s38, 6\n"
    "v_lshl_or_b32 v10, v3, 4, v4\n"
    "s_lshr_b32 s45, s2, 6\n"
    "s_lshl_b32 s32, s45, 14\n"
    "s_add_u32 s32, s32, s38\n"
    "s_lshl_b32 s32, s32, 4\n"
    "s_bfe_u32 s47, s2, 0x30003\n"
    "s_and_b32 s46, s2, 3\n"
    "s_lshl3_add_u32 s46, s46, s47\n"
    "s_lshl3_add_u32 s33, s46, s38\n"
    "s_lshl_b32 s33, s33, 5\n"
    "s_add_u32 s40, s45, s47\n"
    "s_and_b32 s40, s40, 1\n"
    "s_lshl_b32 s40, s40, 3\n"
    "s_lshl_b32 s45, s45, 2\n"
    "v_xor_b32 v11, 64, v10\n"
    "v_add_u32 v11, 0x3c00, v11\n"
    "v_add_u32 v20, 0x7800, v10\n"
    "v_add_u32 v21, 0x7800, v11\n"
    "s_mov_b32 s11, 0x20000\n"
    "s_lshl_b32 s10, s11, 3\n"
    "s_mov_b32 m0, s38\n"
    "s_cmp_lt_i32 s38, 0\n"
    "s_cbranch_scc1 97f\n"
    "s_waitcnt lgkmcnt(0)\n"
    "s_add_u32 s46, s12, s33\n"
    "s_bfe_u32 s46, s46, 0x20008\n"
    "s_cmp_eq_u32 s46, 1\n"
    "s_cselect_b32 s40, s45, s40\n"
    "s_lshl_b32 s45, s40, 7\n"
    "s_add_u32 s45, s45, s32\n"
    "buffer_load_dwordx4 v10, s[8:11], s45 offen offset:0 lds\n"
    "buffer_load_dwordx4 v11, s[8:11], s45 offen offset:1024 lds\n"
    "buffer_load_dwordx4 v20, s[8:11], s45 offen offset:2048 lds\n"
    "buffer_load_dwordx4 v21, s[8:11], s45 offen offset:3072 lds\n"
    "v_and_b32 v3, 15, v1\n"
    "v_lshlrev_b32 v4, 1, v2\n"
    "v_xor_b32 v3, v3, v4\n"
    "v_lshlrev_b32 v4, 13, v2\n"
    "v_lshl_or_b32 v12, v3, 4, v4\n"
    "s_lshl_b32 s14, s11, 6\n"
    "s_mov_b32 s15, s11\n"
    "s_add_u32 m0, s38, 0x4000\n"
    "s_lshl_b32 s45, s40, 19\n"
    "s_add_u32 s45, s45, s33\n"
    "v_add_u32 v22, 0x7c00, v12\n"
    "v_xor_b32 v24, 0x80, v12\n"
    "v_add_u32 v23, 0xf800, v24\n"
    "v_add_u32 v24, 0x17400, v24\n"
    "buffer_load_dwordx4 v12, s[12:15], s45 offen offset:0 lds\n"
    "buffer_load_dwordx4 v22, s[12:15], s45 offen offset:1024 lds\n"
    "buffer_load_dwordx4 v23, s[12:15], s45 offen offset:2048 lds\n"
    "buffer_load_dwordx4 v24, s[12:15], s45 offen offset:3072 lds\n"
    "s_add_u32 s40, s40, 1\n"
    "97:\n"
    "s_load_dwordx2 s[28:29], s[0:1], 88\n"
    "s_load_dwordx2 s[16:17], s[0:1], s44 offset:8\n"
    "s_load_dwordx2 s[20:21], s[0:1], s44 offset:16\n"
    "s_load_dwordx2 s[24:25], s[0:1], s44 offset:32\n"
    "s_load_dwordx2 s[56:57], s[0:1], 80\n"
    "s_cmp_lt_i32 s38, 0\n"
    "s_cbranch_scc1 96f\n"
    "s_lshl_b32 s44, s40, 7\n"
    "s_add_u32 s44, s44, s32\n"
    "s_lshl_b32 s45, s40, 19\n"
    "s_add_u32 s45, s45, s33\n"
    "s_add_u32 s40, s40, 1\n"
    "s_and_b32 s40, s40, 15\n"
    "s_add_u32 m0, s38, 0x8000\n"
    "s_nop 0\n"
    "buffer_load_dwordx4 v10, s[8:11], s44 offen offset:0 lds\n"
    "buffer_load_dwordx4 v11, s[8:11], s44 offen offset:1024 lds\n"
    "buffer_load_dwordx4 v20, s[8:11], s44 offen offset:2048 lds\n"
    "buffer_load_dwordx4 v21, s[8:11], s44 offen offset:3072 lds\n"
    "s_add_u32 m0, s38, 0xc000\n"
    "s_nop 0\n"
    "buffer_load_dwordx4 v12, s[12:15], s45 offen offset:0 lds\n"
    "buffer_load_dwordx4 v22, s[12:15], s45 offen offset:1024 lds\n"
    "buffer_load_dwordx4 v23, s[12:15], s45 offen offset:2048 lds\n"
    "buffer_load_dwordx4 v24, s[12:15], s45 offen offset:3072 lds\n"
    "96:\n"
    : "+{v0}"(tid), "+{s2}"(bidu), "=&{s[8:9]}"(px), "=&{s[16:17]}"(pk), "=&{s[20:21]}"(pv), "=&{s[12:13]}"(pq), "=&{s[24:25]}"(po),
      "=&{s[56:57]}"(pout), "=&{s[28:29]}"(pws), "=&{s[10:11]}"(dx), "=&{s[14:15]}"(dq), "=&{v10}"(vx0), "=&{v11}"(vx1), "=&{v20}"(vx2), "=&{v21}"(vx3),
      "=&{v12}"(vw0), "=&{v22}"(vw1), "=&{v23}"(vw2), "=&{v24}"(vw3), "=&{s[32:33]}"(sxw0), "=&{s38}"(sl), "=&{s40}"(st0)
    : "{s[0:1]}"(__builtin_amdgcn_kernarg_segment_ptr()) : "memory", "scc", "s44", "s45", "s46", "s47", "v1", "v2", "v3", "v4");
  const int wid = __builtin_amdgcn_readfirstlane(tid >> 6);
  const int bid = bidu, xcd = bid & 7, j32 = bid >> 3, br = xcd >> 2, g = xcd & 3, bg = j32 >> 3, j = j32 & 7;
  const int head = 8 * g + j, hh = g >> 1, col0 = 512 * (g & 1) + 64 * j, ro = (bg + j) & 3, grp = (br * 4 + bg) * 2 + hh;
  const unsigned eoff = WS_EPOCH + (br * 4 + bg) * 128;
  {
    const int lw = wid - 4, l = lane_id();
    const unsigned vo0 = (l >> 3) * 2048 + (((l & 7) ^ (((l >> 4) & 1) << 1)) << 4);
    const unsigned vk0 = (l >> 4) * 256 + (((l & 15) ^ (l >> 4)) << 4);
    const unsigned vpoll = WS_BIGF + grp * 8192 + (((l & 15) >> 1) * 8 + (lw >> 1) * 4 + (lw & 1) * 2 + (l & 1)) * 128;
    i32x4 sb;
    sb[0] = ((4 * bg + lw) * 4 + g) * 8192;
    sb[1] = (4 * bg + (lw >> 1)) * 32768 + hh * 16384 + (lw & 1) * 4096;
    sb[2] = (hh * 2048 + 32 * lw) * 2048 + col0 * 2;
    sb[3] = grp * 131072 + lw * 4096;
    const int h0 = 8 * (ro & 1);
    asm volatile(
    "s_cmp_lt_i32 s38, 0\n"
    "s_cbranch_scc1 99f\n"
    "s_mov_b32 s18, 0x80000\n"
    "s_mov_b32 s19, s11\n"
    "s_mov_b32 s22, 0x80000\n"
    "s_mov_b32 s23, s11\n"
    "s_mov_b32 s26, 0x800000\n"
    "s_mov_b32 s27, s11\n"
    "s_mov_b32 s30, 0x900000\n"
    "s_mov_b32 s31, s11\n"
    "v_xor_b32 v28, 0x40, v14\n"
    "v_xor_b32 v29, 0x80, v14\n"
    "v_xor_b32 v30, 0xc0, v14\n"
    "s_waitcnt lgkmcnt(0)\n"
    "buffer_load_dwordx2 v[32:33], off, s[28:31], s39 sc1\n"
    "s_lshl_b32 s46, s38, 9\n"
    "s_load_dword s48, s[12:13], s46\n"
    "s_load_dword s48, s[24:25], s46\n"
    "s_load_dword s48, s[20:21], s35\n"
    "buffer_load_dwordx2 v[34:35], v16, s[28:31], 0 offen sc1\n"
    "s_add_u32 s45, s34, 4096\n"
    "s_lshl_b32 s46, s38, 1\n"
    "s_add_u32 m0, s46, 0x20000\n"
    "s_nop 0\n"
    "buffer_load_dwordx4 v14, s[16:19], s34 offen offset:0 lds\n"
    "buffer_load_dwordx4 v28, s[16:19], s34 offen offset:1024 lds\n"
    "buffer_load_dwordx4 v29, s[16:19], s34 offen offset:2048 lds\n"
    "buffer_load_dwordx4 v30, s[16:19], s34 offen offset:3072 lds\n"
    "s_add_u32 m0, s46, 0x21000\n"
    "s_nop 0\n"
    "buffer_load_dwordx4 v14, s[16:19], s45 offen offset:0 lds\n"
    "buffer_load_dwordx4 v28, s[16:19], s45 offen offset:1024 lds\n"
    "buffer_load_dwordx4 v29, s[16:19], s45 offen offset:2048 lds\n"
    "buffer_load_dwordx4 v30, s[16:19], s45 offen offset:3072 lds\n"
    "v_xor_b32 v27, 0x40, v13\n"
    "v_add_u32 v25, 0x3c00, v27\n"
    "v_add_u32 v26, 0x7800, v13\n"
    "v_add_u32 v27, 0xb400, v27\n"
    "s_waitcnt vmcnt(18)\n"
    "s_barrier\n"
    "s_lshl_b32 s44, s40, 7\n"
    "s_add_u32 s44, s44, s32\n"
    "s_lshl_b32 s45, s40, 19\n"
    "s_add_u32 s45, s45, s33\n"
    "s_add_u32 s40, s40, 1\n"
    "s_and_b32 s40, s40, 15\n"
    "s_add_u32 m0, s38, 0x10000\n"
    "s_nop 0\n"
    "buffer_load_dwordx4 v10, s[8:11], s44 offen offset:0 lds\n"
    "buffer_load_dwordx4 v11, s[8:11], s44 offen offset:1024 lds\n"
    "buffer_load_dwordx4 v20, s[8:11], s44 offen offset:2048 lds\n"
    "buffer_load_dwordx4 v21, s[8:11], s44 offen offset:3072 lds\n"
    "s_add_u32 m0, s38, 0x14000\n"
    "s_nop 0\n"
    "buffer_load_dwordx4 v12, s[12:15], s45 offen offset:0 lds\n"
    "buffer_load_dwordx4 v22, s[12:15], s45 offen offset:1024 lds\n"
    "buffer_load_dwordx4 v23, s[12:15], s45 offen offset:2048 lds\n"
    "buffer_load_dwordx4 v24, s[12:15], s45 offen offset:3072 lds\n"
    "s_lshl_b32 s44, s40, 7\n"
    "s_add_u32 s44, s44, s32\n"
    "s_lshl_b32 s45, s40, 19\n"
    "s_add_u32 s45, s45, s33\n"
    "s_add_u32 s40, s40, 1\n"
    "s_and_b32 s40, s40, 15\n"
    "s_add_u32 m0, s38, 0x18000\n"
    "s_nop 0\n"
    "buffer_load_dwordx4 v10, s[8:11], s44 offen offset:0 lds\n"
    "buffer_load_dwordx4 v11, s[8:11], s44 offen offset:1024 lds\n"
    "buffer_load_dwordx4 v20, s[8:11], s44 offen offset:2048 lds\n"
    "buffer_load_dwordx4 v21, s[8:11], s44 offen offset:3072 lds\n"
    "s_add_u32 m0, s38, 0x1c000\n"
    "s_nop 0\n"
    "buffer_load_dwordx4 v12, s[12:15], s45 offen offset:0 lds\n"
    "buffer_load_dwordx4 v22, s[12:15], s45 offen offset:1024 lds\n"
    "buffer_load_dwordx4 v23, s[12:15], s45 offen offset:2048 lds\n"
    "buffer_load_dwordx4 v24, s[12:15], s45 offen offset:3072 lds\n"
    "s_waitcnt vmcnt(16)\n"
    "s_barrier\n"
    "v_add_u32 v32, 0x7f4a7c15, v32\n"
    "v_add_u32 v33, 0x9e3779b9, v33\n"
    "s_waitcnt vmcnt(8)\n"
    "s_barrier\n"
    "s_lshl_b32 s44, s40, 7\n"
    "s_add_u32 s44, s44, s32\n"
    "s_lshl_b32 s45, s40, 19\n"
    "s_add_u32 s45, s45, s33\n"
    "s_add_u32 s40, s40, 1\n"
    "s_and_b32 s40, s40, 15\n"
    "s_add_u32 m0, s38, 0x20000\n"
    "s_nop 0\n"
    "buffer_load_dwordx4 v10, s[8:11], s44 offen offset:0 lds\n"
    "buffer_load_dwordx4 v11, s[8:11], s44 offen offset:1024 lds\n"
    "buffer_load_dwordx4 v20, s[8:11], s44 offen offset:2048 lds\n"
    "buffer_load_dwordx4 v21, s[8:11], s44 offen offset:3072 lds\n"
    "s_add_u32 m0, s38, 0x24000\n"
    "s_nop 0\n"
    "buffer_load_dwordx4 v12, s[12:15], s45 offen offset:0 lds\n"
    "buffer_load_dwordx4 v22, s[12:15], s45 offen offset:1024 lds\n"
    "buffer_load_dwordx4 v23, s[12:15], s45 offen offset:2048 lds\n"
    "buffer_load_dwordx4 v24, s[12:15], s45 offen offset:3072 lds\n"
    "s_lshl_b32 s44, s40, 7\n"
    "s_add_u32 s44, s44, s32\n"
    "s_lshl_b32 s45, s40, 19\n"
    "s_add_u32 s45, s45, s33\n"
    "s_add_u32 s40, s40, 1\n"
    "s_and_b32 s40, s40, 15\n"
    "s_add_u32 m0, s38, 0x0\n"
    "s_nop 0\n"
    "buffer_load_dwordx4 v10, s[8:11], s44 offen offset:0 lds\n"
    "buffer_load_dwordx4 v11, s[8:11], s44 offen offset:1024 lds\n"
    "buffer_load_dwordx4 v20, s[8:11], s44 offen offset:2048 lds\n"
    "buffer_load_dwordx4 v21, s[8:11], s44 offen offset:3072 lds\n"
    "s_add_u32 m0, s38, 0x4000\n"
    "s_nop 0\n"
    "buffer_load_dwordx4 v12, s[12:15], s45 offen offset:0 lds\n"
    "buffer_load_dwordx4 v22, s[12:15], s45 offen offset:1024 lds\n"
    "buffer_load_dwordx4 v23, s[12:15], s45 offen offset:2048 lds\n"
    "buffer_load_dwordx4 v24, s[12:15], s45 offen offset:3072 lds\n"
    "s_waitcnt vmcnt(16)\n"
    "s_barrier\n"
    "s_lshl_b32 s44, s40, 7\n"
    "s_add_u32 s44, s44, s32\n"
    "s_lshl_b32 s45, s40, 19\n"
    "s_add_u32 s45, s45, s33\n"
    "s_add_u32 s40, s40, 1\n"
    "s_and_b32 s40, s40, 15\n"
    "s_add_u32 m0, s38, 0x8000\n"
    "s_nop 0\n"
    "buffer_load_dwordx4 v10, s[8:11], s44 offen offset:0 lds\n"
    "buffer_load_dwordx4 v11, s[8:11], s44 offen offset:1024 lds\n"
    "buffer_load_dwordx4 v20, s[8:11], s44 offen offset:2048 lds\n"
    "buffer_load_dwordx4 v21, s[8:11], s44 offen offset:3072 lds\n"
    "s_add_u32 m0, s38, 0xc000\n"
    "s_nop 0\n"
    "buffer_load_dwordx4 v12, s[12:15], s45 offen offset:0 lds\n"
    "buffer_load_dwordx4 v22, s[12:15], s45 offen offset:1024 lds\n"
    "buffer_load_dwordx4 v23, s[12:15], s45 offen offset:2048 lds\n"
    "buffer_load_dwordx4 v24, s[12:15], s45 offen offset:3072 lds\n"
    "s_waitcnt vmcnt(16)\n"
    "s_barrier\n"
    "s_lshl_b32 s44, s40, 7\n"
    "s_add_u32 s44, s44, s32\n"
    "s_lshl_b32 s45, s40, 19\n"
    "s_add_u32 s45, s45, s33\n"
    "s_add_u32 s40, s40, 1\n"
    "s_and_b32 s40, s40, 15\n"
    "s_add_u32 m0, s38, 0x10000\n"
    "s_nop 0\n"
    "buffer_load_dwordx4 v10, s[8:11], s44 offen offset:0 lds\n"
    "buffer_load_dwordx4 v11, s[8:11], s44 offen offset:1024 lds\n"
    "buffer_load_dwordx4 v20, s[8:11], s44 offen offset:2048 lds\n"
    "buffer_load_dwordx4 v21, s[8:11], s44 offen offset:3072 lds\n"
    "s_add_u32 m0, s38, 0x14000\n"
    "s_nop 0\n"
    "buffer_load_dwordx4 v12, s[12:15], s45 offen offset:0 lds\n"
    "buffer_load_dwordx4 v22, s[12:15], s45 offen offset:1024 lds\n"
    "buffer_load_dwordx4 v23, s[12:15], s45 offen offset:2048 lds\n"
    "buffer_load_dwordx4 v24, s[12:15], s45 offen offset:3072 lds\n"
    "s_waitcnt vmcnt(16)\n"
    "s_barrier\n"
    "s_lshl_b32 s44, s40, 7\n"
    "s_add_u32 s44, s44, s32\n"
    "s_lshl_b32 s45, s40, 19\n"
    "s_add_u32 s45, s45, s33\n"
    "s_add_u32 s40, s40, 1\n"
    "s_and_b32 s40, s40, 15\n"
    "s_add_u32 m0, s38, 0x18000\n"
    "s_nop 0\n"
    "buffer_load_dwordx4 v10, s[8:11], s44 offen offset:0 lds\n"
    "buffer_load_dwordx4 v11, s[8:11], s44 offen offset:1024 lds\n"
    "buffer_load_dwordx4 v20, s[8:11], s44 offen offset:2048 lds\n"
    "buffer_load_dwordx4 v21, s[8:11], s44 offen offset:3072 lds\n"
    "s_add_u32 m0, s38, 0x1c000\n"
    "s_nop 0\n"
    "buffer_load_dwordx4 v12, s[12:15], s45 offen offset:0 lds\n"
    "buffer_load_dwordx4 v22, s[12:15], s45 offen offset:1024 lds\n"
    "buffer_load_dwordx4 v23, s[12:15], s45 offen offset:2048 lds\n"
    "buffer_load_dwordx4 v24, s[12:15], s45 offen offset:3072 lds\n"
    "s_waitcnt vmcnt(16)\n"
    "s_barrier\n"
    "s_lshl_b32 s44, s40, 7\n"
    "s_add_u32 s44, s44, s32\n"
    "s_lshl_b32 s45, s40, 19\n"
    "s_add_u32 s45, s45, s33\n"
    "s_add_u32 s40, s40, 1\n"
    "s_and_b32 s40, s40, 15\n"
    "s_add_u32 m0, s38, 0x20000\n"
    "s_nop 0\n"
    "buffer_load_dwordx4 v10, s[8:11], s44 offen offset:0 lds\n"
    "buffer_load_dwordx4 v11, s[8:11], s44 offen offset:1024 lds\n"
    "buffer_load_dwordx4 v20, s[8:11], s44 offen offset:2048 lds\n"
    "buffer_load_dwordx4 v21, s[8:11], s44 offen offset:3072 lds\n"
    "s_add_u32 m0, s38, 0x24000\n"
    "s_nop 0\n"
    "buffer_load_dwordx4 v12, s[12:15], s45 offen offset:0 lds\n"
    "buffer_load_dwordx4 v22, s[12:15], s45 offen offset:1024 lds\n"
    "buffer_load_dwordx4 v23, s[12:15], s45 offen offset:2048 lds\n"
    "buffer_load_dwordx4 v24, s[12:15], s45 offen offset:3072 lds\n"
    "s_waitcnt vmcnt(16)\n"
    "s_barrier\n"
    "s_lshl_b32 s44, s40, 7\n"
    "s_add_u32 s44, s44, s32\n"
    "s_lshl_b32 s45, s40, 19\n"
    "s_add_u32 s45, s45, s33\n"
    "s_add_u32 s40, s40, 1\n"
    "s_and_b32 s40, s40, 15\n"
    "s_add_u32 m0, s38, 0x0\n"
    "s_nop 0\n"
    "buffer_load_dwordx4 v10, s[8:11], s44 offen offset:0 lds\n"
    "buffer_load_dwordx4 v11, s[8:11], s44 offen offset:1024 lds\n"
    "buffer_load_dwordx4 v20, s[8:11], s44 offen offset:2048 lds\n"
    "buffer_load_dwordx4 v21, s[8:11], s44 offen offset:3072 lds\n"
    "s_add_u32 m0, s38, 0x4000\n"
    "s_nop 0\n"
    "buffer_load_dwordx4 v12, s[12:15], s45 offen offset:0 lds\n"
    "buffer_load_dwordx4 v22, s[12:15], s45 offen offset:1024 lds\n"
    "buffer_load_dwordx4 v23, s[12:15], s45 offen offset:2048 lds\n"
    "buffer_load_dwordx4 v24, s[12:15], s45 offen offset:3072 lds\n"
    "s_waitcnt vmcnt(16)\n"
    "s_barrier\n"
    "s_lshl_b32 s44, s40, 7\n"
    "s_add_u32 s44, s44, s32\n"
    "s_lshl_b32 s45, s40, 19\n"
    "s_add_u32 s45, s45, s33\n"
    "s_add_u32 s40, s40, 1\n"
    "s_and_b32 s40, s40, 15\n"
    "s_add_u32 m0, s38, 0x8000\n"
    "s_nop 0\n"
    "buffer_load_dwordx4 v10, s[8:11], s44 offen offset:0 lds\n"
    "buffer_load_dwordx4 v11, s[8:11], s44 offen offset:1024 lds\n"
    "buffer_load_dwordx4 v20, s[8:11], s44 offen offset:2048 lds\n"
    "buffer_load_dwordx4 v21, s[8:11], s44 offen offset:3072 lds\n"
    "s_add_u32 m0, s38, 0xc000\n"
    "s_nop 0\n"
    "buffer_load_dwordx4 v12, s[12:15], s45 offen offset:0 lds\n"
    "buffer_load_dwordx4 v22, s[12:15], s45 offen offset:1024 lds\n"
    "buffer_load_dwordx4 v23, s[12:15], s45 offen offset:2048 lds\n"
    "buffer_load_dwordx4 v24, s[12:15], s45 offen offset:3072 lds\n"
    "s_waitcnt vmcnt(16)\n"
    "s_barrier\n"
    "s_lshl_b32 s44, s40, 7\n"
    "s_add_u32 s44, s44, s32\n"
    "s_lshl_b32 s45, s40, 19\n"
    "s_add_u32 s45, s45, s33\n"
    "s_add_u32 s40, s40, 1\n"
    "s_and_b32 s40, s40, 15\n"
    "s_add_u32 m0, s38, 0x10000\n"
    "s_nop 0\n"
    "buffer_load_dwordx4 v10, s[8:11], s44 offen offset:0 lds\n"
    "buffer_load_dwordx4 v11, s[8:11], s44 offen offset:1024 lds\n"
    "buffer_load_dwordx4 v20, s[8:11], s44 offen offset:2048 lds\n"
    "buffer_load_dwordx4 v21, s[8:11], s44 offen offset:3072 lds\n"
    "s_add_u32 m0, s38, 0x14000\n"
    "s_nop 0\n"
    "buffer_load_dwordx4 v12, s[12:15], s45 offen offset:0 lds\n"
    "buffer_load_dwordx4 v22, s[12:15], s45 offen offset:1024 lds\n"
    "buffer_load_dwordx4 v23, s[12:15], s45 offen offset:2048 lds\n"
    "buffer_load_dwordx4 v24, s[12:15], s45 offen offset:3072 lds\n"
    "s_waitcnt vmcnt(16)\n"
    "s_barrier\n"
    "s_lshl_b32 s44, s40, 7\n"
    "s_add_u32 s44, s44, s32\n"
    "s_lshl_b32 s45, s40, 19\n"
    "s_add_u32 s45, s45, s33\n"
    "s_add_u32 s40, s40, 1\n"
    "s_and_b32 s40, s40, 15\n"
    "s_add_u32 m0, s38, 0x18000\n"
    "s_nop 0\n"
    "buffer_load_dwordx4 v10, s[8:11], s44 offen offset:0 lds\n"
    "buffer_load_dwordx4 v11, s[8:11], s44 offen offset:1024 lds\n"
    "buffer_load_dwordx4 v20, s[8:11], s44 offen offset:2048 lds\n"
    "buffer_load_dwordx4 v21, s[8:11], s44 offen offset:3072 lds\n"
    "s_add_u32 m0, s38, 0x1c000\n"
    "s_nop 0\n"
    "buffer_load_dwordx4 v12, s[12:15], s45 offen offset:0 lds\n"
    "buffer_load_dwordx4 v22, s[12:15], s45 offen offset:1024 lds\n"
    "buffer_load_dwordx4 v23, s[12:15], s45 offen offset:2048 lds\n"
    "buffer_load_dwordx4 v24, s[12:15], s45 offen offset:3072 lds\n"
    "s_waitcnt vmcnt(16)\n"
    "s_barrier\n"
    "s_lshl_b32 s44, s40, 7\n"
    "s_add_u32 s44, s44, s32\n"
    "s_lshl_b32 s45, s40, 19\n"
    "s_add_u32 s45, s45, s33\n"
    "s_add_u32 s40, s40, 1\n"
    "s_and_b32 s40, s40, 15\n"
    "s_add_u32 m0, s38, 0x20000\n"
    "s_nop 0\n"
    "buffer_load_dwordx4 v10, s[8:11], s44 offen offset:0 lds\n"
    "buffer_load_dwordx4 v11, s[8:11], s44 offen offset:1024 lds\n"
    "buffer_load_dwordx4 v20, s[8:11], s44 offen offset:2048 lds\n"
    "buffer_load_dwordx4 v21, s[8:11], s44 offen offset:3072 lds\n"
    "s_add_u32 m0, s38, 0x24000\n"
    "s_nop 0\n"
    "buffer_load_dwordx4 v12, s[12:15], s45 offen offset:0 lds\n"
    "buffer_load_dwordx4 v22, s[12:15], s45 offen offset:1024 lds\n"
    "buffer_load_dwordx4 v23, s[12:15], s45 offen offset:2048 lds\n"
    "buffer_load_dwordx4 v24, s[12:15], s45 offen offset:3072 lds\n"
    "s_waitcnt vmcnt(16)\n"
    "s_barrier\n"
    "s_lshl_b32 s44, s40, 7\n"
    "s_add_u32 s44, s44, s32\n"
    "s_lshl_b32 s45, s40, 19\n"
    "s_add_u32 s45, s45, s33\n"
    "s_add_u32 s40, s40, 1\n"
    "s_and_b32 s40, s40, 15\n"
    "s_add_u32 m0, s38, 0x0\n"
    "s_nop 0\n"
    "buffer_load_dwordx4 v10, s[8:11], s44 offen offset:0 lds\n"
    "buffer_load_dwordx4 v11, s[8:11], s44 offen offset:1024 lds\n"
    "buffer_load_dwordx4 v20, s[8:11], s44 offen offset:2048 lds\n"
    "buffer_load_dwordx4 v21, s[8:11], s44 offen offset:3072 lds\n"
    "s_add_u32 m0, s38, 0x4000\n"
    "s_nop 0\n"
    "buffer_load_dwordx4 v12, s[12:15], s45 offen offset:0 lds\n"
    "buffer_load_dwordx4 v22, s[12:15], s45 offen offset:1024 lds\n"
    "buffer_load_dwordx4 v23, s[12:15], s45 offen offset:2048 lds\n"
    "buffer_load_dwordx4 v24, s[12:15], s45 offen offset:3072 lds\n"
    "s_waitcnt vmcnt(16)\n"
    "s_barrier\n"
    "s_add_u32 s44, s42, 0\n"
    "s_and_b32 s44, s44, 15\n"
    "s_lshl_b32 s44, s44, 18\n"
    "s_add_u32 s44, s44, s36\n"
    "s_add_u32 m0, s38, 0x8000\n"
    "s_nop 0\n"
    "buffer_load_dwordx4 v13, s[24:27], s44 offen offset:0 lds\n"
    "buffer_load_dwordx4 v25, s[24:27], s44 offen offset:1024 lds\n"
    "buffer_load_dwordx4 v26, s[24:27], s44 offen offset:2048 lds\n"
    "buffer_load_dwordx4 v27, s[24:27], s44 offen offset:3072 lds\n"
    "s_add_u32 s44, s41, 0\n"
    "s_and_b32 s44, s44, 15\n"
    "s_lshl_b32 s44, s44, 10\n"
    "s_add_u32 s44, s44, s35\n"
    "s_add_u32 m0, s38, 0xc000\n"
    "s_nop 0\n"
    "buffer_load_dwordx4 v14, s[20:23], s44 offen offset:0 lds\n"
    "buffer_load_dwordx4 v28, s[20:23], s44 offen offset:1024 lds\n"
    "buffer_load_dwordx4 v29, s[20:23], s44 offen offset:2048 lds\n"
    "buffer_load_dwordx4 v30, s[20:23], s44 offen offset:3072 lds\n"
    "s_waitcnt vmcnt(16)\n"
    "s_barrier\n"
    "s_add_u32 s44, s42, 1\n"
    "s_and_b32 s44, s44, 15\n"
    "s_lshl_b32 s44, s44, 18\n"
    "s_add_u32 s44, s44, s36\n"
    "s_add_u32 m0, s38, 0x10000\n"
    "s_nop 0\n"
    "buffer_load_dwordx4 v13, s[24:27], s44 offen offset:0 lds\n"
    "buffer_load_dwordx4 v25, s[24:27], s44 offen offset:1024 lds\n"
    "buffer_load_dwordx4 v26, s[24:27], s44 offen offset:2048 lds\n"
    "buffer_load_dwordx4 v27, s[24:27], s44 offen offset:3072 lds\n"
    "s_waitcnt vmcnt(12)\n"
    "s_barrier\n"
    "s_add_u32 s44, s41, 0\n"
    "s_and_b32 s44, s44, 15\n"
    "s_lshl_b32 s44, s44, 10\n"
    "s_add_u32 s44, s44, s35\n"
    "s_add_u32 s44, s44, 0x10000\n"
    "s_add_u32 m0, s38, 0x14000\n"
    "s_nop 0\n"
    "buffer_load_dwordx4 v14, s[20:23], s44 offen offset:0 lds\n"
    "buffer_load_dwordx4 v28, s[20:23], s44 offen offset:1024 lds\n"
    "buffer_load_dwordx4 v29, s[20:23], s44 offen offset:2048 lds\n"
    "buffer_load_dwordx4 v30, s[20:23], s44 offen offset:3072 lds\n"
    "s_barrier\n"
    "s_add_u32 s44, s42, 2\n"
    "s_and_b32 s44, s44, 15\n"
    "s_lshl_b32 s44, s44, 18\n"
    "s_add_u32 s44, s44, s36\n"
    "s_add_u32 m0, s38, 0x18000\n"
    "s_nop 0\n"
    "buffer_load_dwordx4 v13, s[24:27], s44 offen offset:0 lds\n"
    "buffer_load_dwordx4 v25, s[24:27], s44 offen offset:1024 lds\n"
    "buffer_load_dwordx4 v26, s[24:27], s44 offen offset:2048 lds\n"
    "buffer_load_dwordx4 v27, s[24:27], s44 offen offset:3072 lds\n"
    "s_barrier\n"
    "s_waitcnt vmcnt(4)\n"
    "s_barrier\n"
    "s_add_u32 s44, s43, 4\n"
    "s_and_b32 s44, s44, 15\n"
    "s_lshl_b32 s44, s44, 18\n"
    "s_add_u32 s44, s44, s36\n"
    "s_add_u32 m0, s38, 0x1c000\n"
    "s_nop 0\n"
    "buffer_load_dwordx4 v13, s[24:27], s44 offen offset:0 lds\n"
    "buffer_load_dwordx4 v25, s[24:27], s44 offen offset:1024 lds\n"
    "buffer_load_dwordx4 v26, s[24:27], s44 offen offset:2048 lds\n"
    "buffer_load_dwordx4 v27, s[24:27], s44 offen offset:3072 lds\n"
    "s_add_u32 s44, s43, 5\n"
    "s_and_b32 s44, s44, 15\n"
    "s_lshl_b32 s44, s44, 18\n"
    "s_add_u32 s44, s44, s36\n"
    "s_add_u32 m0, s38, 0x20000\n"
    "s_nop 0\n"
    "buffer_load_dwordx4 v13, s[24:27], s44 offen offset:0 lds\n"
    "buffer_load_dwordx4 v25, s[24:27], s44 offen offset:1024 lds\n"
    "buffer_load_dwordx4 v26, s[24:27], s44 offen offset:2048 lds\n"
    "buffer_load_dwordx4 v27, s[24:27], s44 offen offset:3072 lds\n"
    "s_add_u32 s44, s42, 3\n"
    "s_and_b32 s44, s44, 15\n"
    "s_lshl_b32 s44, s44, 18\n"
    "s_add_u32 s44, s44, s36\n"
    "s_add_u32 m0, s38, 0x24000\n"
    "s_nop 0\n"
    "buffer_load_dwordx4 v13, s[24:27], s44 offen offset:0 lds\n"
    "buffer_load_dwordx4 v25, s[24:27], s44 offen offset:1024 lds\n"
    "buffer_load_dwordx4 v26, s[24:27], s44 offen offset:2048 lds\n"
    "buffer_load_dwordx4 v27, s[24:27], s44 offen offset:3072 lds\n"
    "s_waitcnt vmcnt(8)\n"
    "s_barrier\n"
    "s_waitcnt vmcnt(4)\n"
    "s_barrier\n"
    "s_add_u32 s44, s43, 6\n"
    "s_and_b32 s44, s44, 15\n"
    "s_lshl_b32 s44, s44, 18\n"
    "s_add_u32 s44, s44, s36\n"
    "s_add_u32 m0, s38, 0x8000\n"
    "s_nop 0\n"
    "buffer_load_dwordx4 v13, s[24:27], s44 offen offset:0 lds\n"
    "buffer_load_dwordx4 v25, s[24:27], s44 offen offset:1024 lds\n"
    "buffer_load_dwordx4 v26, s[24:27], s44 offen offset:2048 lds\n"
    "buffer_load_dwordx4 v27, s[24:27], s44 offen offset:3072 lds\n"
    "s_add_u32 s44, s43, 7\n"
    "s_and_b32 s44, s44, 15\n"
    "s_lshl_b32 s44, s44, 18\n"
    "s_add_u32 s44, s44, s36\n"
    "s_add_u32 m0, s38, 0xc000\n"
    "s_nop 0\n"
    "buffer_load_dwordx4 v13, s[24:27], s44 offen offset:0 lds\n"
    "buffer_load_dwordx4 v25, s[24:27], s44 offen offset:1024 lds\n"
    "buffer_load_dwordx4 v26, s[24:27], s44 offen offset:2048 lds\n"
    "buffer_load_dwordx4 v27, s[24:27], s44 offen offset:3072 lds\n"
    "s_waitcnt vmcnt(8)\n"
    "s_barrier\n"
    "s_mov_b32 s47, 0x40000\n"
    "6:\n"
    "buffer_load_dwordx2 v[34:35], v16, s[28:31], 0 offen sc1\n"
    "s_waitcnt vmcnt(0)\n"
    "v_cmp_eq_u64 vcc, v[34:35], v[32:33]\n"
    "s_sub_u32 s47, s47, 1\n"
    "s_nop 4\n"
    "s_cmp_eq_u64 vcc, -1\n"
    "s_cbranch_scc1 7f\n"
    "s_cmp_lg_u32 s47, 0\n"
    "s_cbranch_scc1 6b\n"
    "7:\n"
    "s_add_u32 s44, s42, 0\n"
    "s_and_b32 s44, s44, 15\n"
    "s_lshl_b32 s44, s44, 13\n"
    "s_add_u32 s44, s44, s37\n"
    "s_add_u32 m0, s38, 0x0\n"
    "s_nop 0\n"
    "buffer_load_dwordx4 v15, s[28:31], s44 offen offset:0 sc1 lds\n"
    "buffer_load_dwordx4 v15, s[28:31], s44 offen offset:1024 sc1 lds\n"
    "buffer_load_dwordx4 v15, s[28:31], s44 offen offset:2048 sc1 lds\n"
    "buffer_load_dwordx4 v15, s[28:31], s44 offen offset:3072 sc1 lds\n"
    "s_add_u32 s44, s41, 8\n"
    "s_and_b32 s44, s44, 15\n"
    "s_lshl_b32 s44, s44, 10\n"
    "s_add_u32 s44, s44, s35\n"
    "s_add_u32 m0, s38, 0x10000\n"
    "s_nop 0\n"
    "buffer_load_dwordx4 v14, s[20:23], s44 offen offset:0 lds\n"
    "buffer_load_dwordx4 v28, s[20:23], s44 offen offset:1024 lds\n"
    "buffer_load_dwordx4 v29, s[20:23], s44 offen offset:2048 lds\n"
    "buffer_load_dwordx4 v30, s[20:23], s44 offen offset:3072 lds\n"
    "s_add_u32 s44, s41, 8\n"
    "s_and_b32 s44, s44, 15\n"
    "s_lshl_b32 s44, s44, 10\n"
    "s_add_u32 s44, s44, s35\n"
    "s_add_u32 s44, s44, 0x10000\n"
    "s_add_u32 m0, s38, 0x14000\n"
    "s_nop 0\n"
    "buffer_load_dwordx4 v14, s[20:23], s44 offen offset:0 lds\n"
    "buffer_load_dwordx4 v28, s[20:23], s44 offen offset:1024 lds\n"
    "buffer_load_dwordx4 v29, s[20:23], s44 offen offset:2048 lds\n"
    "buffer_load_dwordx4 v30, s[20:23], s44 offen offset:3072 lds\n"
    "s_add_u32 s44, s43, 4\n"
    "s_and_b32 s44, s44, 15\n"
    "s_lshl_b32 s44, s44, 13\n"
    "s_add_u32 s44, s44, s37\n"
    "s_add_u32 m0, s38, 0x4000\n"
    "s_nop 0\n"
    "buffer_load_dwordx4 v15, s[28:31], s44 offen offset:0 sc1 lds\n"
    "buffer_load_dwordx4 v15, s[28:31], s44 offen offset:1024 sc1 lds\n"
    "buffer_load_dwordx4 v15, s[28:31], s44 offen offset:2048 sc1 lds\n"
    "buffer_load_dwordx4 v15, s[28:31], s44 offen offset:3072 sc1 lds\n"
    "s_waitcnt vmcnt(12)\n"
    "s_barrier\n"
    "s_add_u32 s44, s42, 2\n"
    "s_and_b32 s44, s44, 15\n"
    "s_lshl_b32 s44, s44, 13\n"
    "s_add_u32 s44, s44, s37\n"
    "s_add_u32 m0, s38, 0x18000\n"
    "s_nop 0\n"
    "buffer_load_dwordx4 v15, s[28:31], s44 offen offset:0 sc1 lds\n"
    "buffer_load_dwordx4 v15, s[28:31], s44 offen offset:1024 sc1 lds\n"
    "buffer_load_dwordx4 v15, s[28:31], s44 offen offset:2048 sc1 lds\n"
    "buffer_load_dwordx4 v15, s[28:31], s44 offen offset:3072 sc1 lds\n"
    "s_add_u32 s44, s42, 8\n"
    "s_and_b32 s44, s44, 15\n"
    "s_lshl_b32 s44, s44, 18\n"
    "s_add_u32 s44, s44, s36\n"
    "s_add_u32 m0, s38, 0x1c000\n"
    "s_nop 0\n"
    "buffer_load_dwordx4 v13, s[24:27], s44 offen offset:0 lds\n"
    "buffer_load_dwordx4 v25, s[24:27], s44 offen offset:1024 lds\n"
    "buffer_load_dwordx4 v26, s[24:27], s44 offen offset:2048 lds\n"
    "buffer_load_dwordx4 v27, s[24:27], s44 offen offset:3072 lds\n"
    "s_add_u32 s44, s43, 6\n"
    "s_and_b32 s44, s44, 15\n"
    "s_lshl_b32 s44, s44, 13\n"
    "s_add_u32 s44, s44, s37\n"
    "s_add_u32 m0, s38, 0x20000\n"
    "s_nop 0\n"
    "buffer_load_dwordx4 v15, s[28:31], s44 offen offset:0 sc1 lds\n"
    "buffer_load_dwordx4 v15, s[28:31], s44 offen offset:1024 sc1 lds\n"
    "buffer_load_dwordx4 v15, s[28:31], s44 offen offset:2048 sc1 lds\n"
    "buffer_load_dwordx4 v15, s[28:31], s44 offen offset:3072 sc1 lds\n"
    "s_waitcnt vmcnt(12)\n"
    "s_barrier\n"
    "s_add_u32 s44, s42, 9\n"
    "s_and_b32 s44, s44, 15\n"
    "s_lshl_b32 s44, s44, 18\n"
    "s_add_u32 s44, s44, s36\n"
    "s_add_u32 m0, s38, 0x24000\n"
    "s_nop 0\n"
    "buffer_load_dwordx4 v13, s[24:27], s44 offen offset:0 lds\n"
    "buffer_load_dwordx4 v25, s[24:27], s44 offen offset:1024 lds\n"
    "buffer_load_dwordx4 v26, s[24:27], s44 offen offset:2048 lds\n"
    "buffer_load_dwordx4 v27, s[24:27], s44 offen offset:3072 lds\n"
    "s_add_u32 s44, s42, 8\n"
    "s_and_b32 s44, s44, 15\n"
    "s_lshl_b32 s44, s44, 13\n"
    "s_add_u32 s44, s44, s37\n"
    "s_add_u32 m0, s38, 0x0\n"
    "s_nop 0\n"
    "buffer_load_dwordx4 v15, s[28:31], s44 offen offset:0 sc1 lds\n"
    "buffer_load_dwordx4 v15, s[28:31], s44 offen offset:1024 sc1 lds\n"
    "buffer_load_dwordx4 v15, s[28:31], s44 offen offset:2048 sc1 lds\n"
    "buffer_load_dwordx4 v15, s[28:31], s44 offen offset:3072 sc1 lds\n"
    "s_waitcnt vmcnt(12)\n"
    "s_barrier\n"
    "s_add_u32 s44, s42, 10\n"
    "s_and_b32 s44, s44, 15\n"
    "s_lshl_b32 s44, s44, 18\n"
    "s_add_u32 s44, s44, s36\n"
    "s_add_u32 m0, s38, 0x8000\n"
    "s_nop 0\n"
    "buffer_load_dwordx4 v13, s[24:27], s44 offen offset:0 lds\n"
    "buffer_load_dwordx4 v25, s[24:27], s44 offen offset:1024 lds\n"
    "buffer_load_dwordx4 v26, s[24:27], s44 offen offset:2048 lds\n"
    "buffer_load_dwordx4 v27, s[24:27], s44 offen offset:3072 lds\n"
    "s_add_u32 s44, s42, 10\n"
    "s_and_b32 s44, s44, 15\n"
    "s_lshl_b32 s44, s44, 13\n"
    "s_add_u32 s44, s44, s37\n"
    "s_add_u32 m0, s38, 0x4000\n"
    "s_nop 0\n"
    "buffer_load_dwordx4 v15, s[28:31], s44 offen offset:0 sc1 lds\n"
    "buffer_load_dwordx4 v15, s[28:31], s44 offen offset:1024 sc1 lds\n"
    "buffer_load_dwordx4 v15, s[28:31], s44 offen offset:2048 sc1 lds\n"
    "buffer_load_dwordx4 v15, s[28:31], s44 offen offset:3072 sc1 lds\n"
    "s_waitcnt vmcnt(4)\n"
    "s_barrier\n"
    "s_add_u32 s44, s42, 11\n"
    "s_and_b32 s44, s44, 15\n"
    "s_lshl_b32 s44, s44, 18\n"
    "s_add_u32 s44, s44, s36\n"
    "s_add_u32 m0, s38, 0xc000\n"
    "s_nop 0\n"
    "buffer_load_dwordx4 v13, s[24:27], s44 offen offset:0 lds\n"
    "buffer_load_dwordx4 v25, s[24:27], s44 offen offset:1024 lds\n"
    "buffer_load_dwordx4 v26, s[24:27], s44 offen offset:2048 lds\n"
    "buffer_load_dwordx4 v27, s[24:27], s44 offen offset:3072 lds\n"
    "s_add_u32 s44, s43, 12\n"
    "s_and_b32 s44, s44, 15\n"
    "s_lshl_b32 s44, s44, 18\n"
    "s_add_u32 s44, s44, s36\n"
    "s_add_u32 m0, s38, 0x10000\n"
    "s_nop 0\n"
    "buffer_load_dwordx4 v13, s[24:27], s44 offen offset:0 lds\n"
    "buffer_load_dwordx4 v25, s[24:27], s44 offen offset:1024 lds\n"
    "buffer_load_dwordx4 v26, s[24:27], s44 offen offset:2048 lds\n"
    "buffer_load_dwordx4 v27, s[24:27], s44 offen offset:3072 lds\n"
    "s_add_u32 s44, s43, 12\n"
    "s_and_b32 s44, s44, 15\n"
    "s_lshl_b32 s44, s44, 13\n"
    "s_add_u32 s44, s44, s37\n"
    "s_add_u32 m0, s38, 0x14000\n"
    "s_nop 0\n"
    "buffer_load_dwordx4 v15, s[28:31], s44 offen offset:0 sc1 lds\n"
    "buffer_load_dwordx4 v15, s[28:31], s44 offen offset:1024 sc1 lds\n"
    "buffer_load_dwordx4 v15, s[28:31], s44 offen offset:2048 sc1 lds\n"
    "buffer_load_dwordx4 v15, s[28:31], s44 offen offset:3072 sc1 lds\n"
    "s_waitcnt vmcnt(4)\n"
    "s_barrier\n"
    "s_add_u32 s44, s43, 13\n"
    "s_and_b32 s44, s44, 15\n"
    "s_lshl_b32 s44, s44, 18\n"
    "s_add_u32 s44, s44, s36\n"
    "s_add_u32 m0, s38, 0x18000\n"
    "s_nop 0\n"
    "buffer_load_dwordx4 v13, s[24:27], s44 offen offset:0 lds\n"
    "buffer_load_dwordx4 v25, s[24:27], s44 offen offset:1024 lds\n"
    "buffer_load_dwordx4 v26, s[24:27], s44 offen offset:2048 lds\n"
    "buffer_load_dwordx4 v27, s[24:27], s44 offen offset:3072 lds\n"
    "s_add_u32 s44, s43, 14\n"
    "s_and_b32 s44, s44, 15\n"
    "s_lshl_b32 s44, s44, 18\n"
    "s_add_u32 s44, s44, s36\n"
    "s_add_u32 m0, s38, 0x1c000\n"
    "s_nop 0\n"
    "buffer_load_dwordx4 v13, s[24:27], s44 offen offset:0 lds\n"
    "buffer_load_dwordx4 v25, s[24:27], s44 offen offset:1024 lds\n"
    "buffer_load_dwordx4 v26, s[24:27], s44 offen offset:2048 lds\n"
    "buffer_load_dwordx4 v27, s[24:27], s44 offen offset:3072 lds\n"
    "s_add_u32 s44, s43, 15\n"
    "s_and_b32 s44, s44, 15\n"
    "s_lshl_b32 s44, s44, 18\n"
    "s_add_u32 s44, s44, s36\n"
    "s_add_u32 m0, s38, 0x20000\n"
    "s_nop 0\n"
    "buffer_load_dwordx4 v13, s[24:27], s44 offen offset:0 lds\n"
    "buffer_load_dwordx4 v25, s[24:27], s44 offen offset:1024 lds\n"
    "buffer_load_dwordx4 v26, s[24:27], s44 offen offset:2048 lds\n"
    "buffer_load_dwordx4 v27, s[24:27], s44 offen offset:3072 lds\n"
    "s_waitcnt vmcnt(4)\n"
    "s_barrier\n"
    "s_add_u32 s44, s43, 14\n"
    "s_and_b32 s44, s44, 15\n"
    "s_lshl_b32 s44, s44, 13\n"
    "s_add_u32 s44, s44, s37\n"
    "s_add_u32 m0, s38, 0x0\n"
    "s_nop 0\n"
    "buffer_load_dwordx4 v15, s[28:31], s44 offen offset:0 sc1 lds\n"
    "buffer_load_dwordx4 v15, s[28:31], s44 offen offset:1024 sc1 lds\n"
    "buffer_load_dwordx4 v15, s[28:31], s44 offen offset:2048 sc1 lds\n"
    "buffer_load_dwordx4 v15, s[28:31], s44 offen offset:3072 sc1 lds\n"
    "s_waitcnt vmcnt(0)\n"
    "s_barrier\n"
    "s_waitcnt lgkmcnt(0)\n"
    "s_endpgm\n"
    "99:\n"
    : "+{s40}"(st0)
    : "{v10}"(vx0), "{v11}"(vx1), "{v20}"(vx2), "{v21}"(vx3), "{v12}"(vw0), "{v22}"(vw1), "{v23}"(vw2), "{v24}"(vw3),
      "{v13}"(vo0), "{v14}"(vk0), "{v15}"((unsigned)(l * 16)), "{v16}"(vpoll),
      "{s[8:11]}"(join(px, dx)), "{s[12:15]}"(join(pq, dq)), "{s[16:17]}"(pk), "{s[20:21]}"(pv), "{s[24:25]}"(po), "{s[28:29]}"(pws), "{s[56:57]}"(pout),
      "{s[32:33]}"(sxw0), "{s[34:35]}"(pair(sb[0], sb[1])), "{s[36:37]}"(pair(sb[2], sb[3])), "{s38}"(sl), "{s39}"(eoff), "{s41}"(h0), "{s[42:43]}"(pair(h0 + 4 * (ro >> 1), h0 + 16 - 4 * (ro >> 1)))
    : "memory", "scc", "vcc", "s44", "s45", "s46", "s47", "s48", "s49", "s50", "s51", "s52", "s53", "s18", "s19", "s22", "s23", "s26", "s27", "s30", "s31", "v25","v26","v27","v28","v29","v30","v31","v32","v33","v34","v35","v36","v37","v38","v39","v40","v41");
  }
  {
    const int l = lane_id();
    const int wd = wid & 1, wq = wid >> 1, r = l & 15, gq = l >> 4, q = (l & 15) >> 2, p = l & 3;
    const unsigned awb = 16384 + 256 * (8 * gq + q) + 16 * (p >> 1) + 8 * (p & 1), sw = q | ((gq & 1) << 2);
    unsigned aw0 = awb + 32 * ((4 * wd + 0) ^ sw), aw1 = awb + 32 * ((4 * wd + 1) ^ sw);
    unsigned aw2 = awb + 32 * ((4 * wd + 2) ^ sw), aw3 = awb + 32 * ((4 * wd + 3) ^ sw);
    unsigned ax0 = 128 * (64 * wq + r) + 16 * (gq ^ (r >> 1));
    unsigned ax1 = ax0 ^ 64;
    const unsigned kb = 131072 + 2 * wq * 8192 + 256 * r + 16 * ((8 * wd + (gq >> 1)) ^ r) + 8 * (gq & 1);
    const unsigned sxw = 147456 + wid * 4096 + l * 16;
    const int pidx = (grp * 16 + (head & 15)) * 4 + wid;
    const unsigned pst = pidx * 2048 + l * 16;
    const unsigned pfl = l == 0 ? WS_BIGF + pidx * 128 : 0xfffffff0u;
    const unsigned wob = 32768 + 128 * (8 * gq + q) + 32 * ((2 * (hh ^ 1)) ^ ((q >> 1) | ((gq & 1) << 1))) + 16 * (p >> 1) + 8 * (p & 1);
    const unsigned vb = (wid & 1) * 8192 + 256 * r + 16 * (gq ^ r);
    const unsigned prd = wid * 2048 + l * 16;
    const unsigned oo = (((4 * bg + wid) * 32 + r) * 2048 + br * 1024 + col0 + 32 * hh + 16 * (gq & 1) + 8 * (gq >> 1)) * 2;
    const int me = bid * 4 + wid, other = (bid ^ 2) * 4 + wid;
    const unsigned xst = WS_PART + me * 4096 + l * 16, xld = WS_PART + other * 4096 + l * 16;
    const unsigned xfl = l == 0 ? WS_BIGX + me * 128 : 0xfffffff0u;
    i32x4 sc;
    sc[0] = 0x10000; sc[1] = wid >> 1; sc[2] = WS_BIGX + other * 128; sc[3] = (g == 0 && j == 0 && wid == 0) ? eoff : 0xfffffff0u;
    asm volatile(
    "s_mov_b32 s30, 0x900000\n"
    "s_mov_b32 s31, 0x20000\n"
    "s_mov_b32 s58, 0x200000\n"
    "s_mov_b32 s59, 0x20000\n"
    "s_getpc_b64 s[96:97]\n"
    "90:\n"
    "s_sub_u32 s96, s96, 90b-98b\n"
    "s_subb_u32 s97, s97, 0\n"
    "s_and_b32 s97, s97, 0xffff\n"
    "s_mov_b32 s98, 91f-98b\n"
    "s_mov_b32 s99, 0x20000\n"
    "v_mbcnt_lo_u32_b32 v0, -1, 0\n"
    "v_mbcnt_hi_u32_b32 v0, -1, v0\n"
    "v_lshlrev_b32 v0, 12, v0\n"
    "buffer_load_dword v1, v0, s[96:99], s95 offen\n"
    "v_mov_b32 v0, 0\n"
    "v_mov_b32 v2, 0\n"
    "v_mov_b32 v3, 0\n"
    "v_mov_b32 v4, 0\n"
    "v_mov_b32 v5, 0\n"
    "v_mov_b32 v6, 0\n"
    "v_mov_b32 v7, 0\n"
    "v_mov_b32 v8, 0\n"
    "v_mov_b32 v9, 0\n"
    "v_mov_b32 v10, 0\n"
    "v_mov_b32 v11, 0\n"
    "v_mov_b32 v12, 0\n"
    "v_mov_b32 v13, 0\n"
    "v_mov_b32 v14, 0\n"
    "v_mov_b32 v15, 0\n"
    "v_mov_b32 v16, 0\n"
    "v_mov_b32 v17, 0\n"
    "v_mov_b32 v18, 0\n"
    "v_mov_b32 v19, 0\n"
    "v_mov_b32 v20, 0\n"
    "v_mov_b32 v21, 0\n"
    "v_mov_b32 v22, 0\n"
    "v_mov_b32 v23, 0\n"
    "v_mov_b32 v24, 0\n"
    "v_mov_b32 v25, 0\n"
    "v_mov_b32 v26, 0\n"
    "v_mov_b32 v27, 0\n"
    "v_mov_b32 v28, 0\n"
    "v_mov_b32 v29, 0\n"
    "v_mov_b32 v30, 0\n"
    "v_mov_b32 v31, 0\n"
    "v_mov_b32 v32, 0\n"
    "v_mov_b32 v33, 0\n"
    "v_mov_b32 v34, 0\n"
    "v_mov_b32 v35, 0\n"
    "v_mov_b32 v36, 0\n"
    "v_mov_b32 v37, 0\n"
    "v_mov_b32 v38, 0\n"
    "v_mov_b32 v39, 0\n"
    "v_mov_b32 v40, 0\n"
    "v_mov_b32 v41, 0\n"
    "v_mov_b32 v42, 0\n"
    "v_mov_b32 v43, 0\n"
    "v_mov_b32 v44, 0\n"
    "v_mov_b32 v45, 0\n"
    "v_mov_b32 v46, 0\n"
    "v_mov_b32 v47, 0\n"
    "v_mov_b32 v48, 0\n"
    "v_mov_b32 v49, 0\n"
    "v_mov_b32 v50, 0\n"
    "v_mov_b32 v51, 0\n"
    "v_mov_b32 v52, 0\n"
    "v_mov_b32 v53, 0\n"
    "v_mov_b32 v54, 0\n"
    "v_mov_b32 v55, 0\n"
    "v_mov_b32 v56, 0\n"
    "v_mov_b32 v57, 0\n"
    "v_mov_b32 v58, 0\n"
    "v_mov_b32 v59, 0\n"
    "v_mov_b32 v60, 0\n"
    "v_mov_b32 v61, 0\n"
    "v_mov_b32 v62, 0\n"
    "v_mov_b32 v63, 0\n"
    "s_waitcnt lgkmcnt(0)\n"
    "s_waitcnt vmcnt(0)\n"
    "v_mov_b32 v1, 0\n"
    "buffer_load_dwordx2 v[214:215], off, s[28:31], s60 sc1\n"
    "buffer_load_dwordx2 v[250:251], off, s[28:31], s70 sc1\n"
    "s_mov_b32 s62, 14\n"
    "s_mov_b32 s63, 0\n"
    "s_mov_b32 s66, 0x8000\n"
    "s_mov_b32 s65, 0x3e0293ee\n"
    "v_xor_b32 v252, 0x20, v206\n"
    "v_xor_b32 v253, 0x40, v206\n"
    "v_xor_b32 v254, 0x60, v206\n"
    "s_barrier\n"
    "ds_read_b64_tr_b16 v[64:65], v200 offset:0\n"
    "ds_read_b64_tr_b16 v[66:67], v200 offset:1024\n"
    "ds_read_b128 v[80:83], v204 offset:0\n"
    "ds_read_b128 v[84:87], v204 offset:2048\n"
    "ds_read_b128 v[88:91], v204 offset:4096\n"
    "ds_read_b128 v[92:95], v204 offset:6144\n"
    "ds_read_b64_tr_b16 v[68:69], v201 offset:0\n"
    "ds_read_b64_tr_b16 v[70:71], v201 offset:1024\n"
    "ds_read_b64_tr_b16 v[72:73], v202 offset:0\n"
    "ds_read_b64_tr_b16 v[74:75], v202 offset:1024\n"
    "ds_read_b64_tr_b16 v[76:77], v203 offset:0\n"
    "ds_read_b64_tr_b16 v[78:79], v203 offset:1024\n"
    "s_waitcnt lgkmcnt(6)\n"
    "v_mfma_f32_16x16x32_bf16 v[0:3], v[64:67], v[80:83], v[0:3]\n"
    "ds_read_b64_tr_b16 v[96:97], v200 offset:8192\n"
    "v_mfma_f32_16x16x32_bf16 v[4:7], v[64:67], v[84:87], v[4:7]\n"
    "ds_read_b64_tr_b16 v[98:99], v200 offset:9216\n"
    "v_mfma_f32_16x16x32_bf16 v[8:11], v[64:67], v[88:91], v[8:11]\n"
    "ds_read_b128 v[112:115], v205 offset:0\n"
    "v_mfma_f32_16x16x32_bf16 v[12:15], v[64:67], v[92:95], v[12:15]\n"
    "ds_read_b128 v[116:119], v205 offset:2048\n"
    "s_waitcnt lgkmcnt(8)\n"
    "v_mfma_f32_16x16x32_bf16 v[28:31], v[68:71], v[92:95], v[28:31]\n"
    "ds_read_b128 v[120:123], v205 offset:4096\n"
    "v_mfma_f32_16x16x32_bf16 v[24:27], v[68:71], v[88:91], v[24:27]\n"
    "ds_read_b128 v[124:127], v205 offset:6144\n"
    "s_add_u32 s63, s63, 1\n"
    "v_mfma_f32_16x16x32_bf16 v[20:23], v[68:71], v[84:87], v[20:23]\n"
    "ds_read_b64_tr_b16 v[100:101], v201 offset:8192\n"
    "s_cmp_eq_u32 s63, 5\n"
    "v_mfma_f32_16x16x32_bf16 v[16:19], v[68:71], v[80:83], v[16:19]\n"
    "ds_read_b64_tr_b16 v[102:103], v201 offset:9216\n"
    "s_waitcnt lgkmcnt(10)\n"
    "v_mfma_f32_16x16x32_bf16 v[32:35], v[72:75], v[80:83], v[32:35]\n"
    "ds_read_b64_tr_b16 v[104:105], v202 offset:8192\n"
    "v_mfma_f32_16x16x32_bf16 v[36:39], v[72:75], v[84:87], v[36:39]\n"
    "ds_read_b64_tr_b16 v[106:107], v202 offset:9216\n"
    "s_cselect_b32 s64, 0xfffe0000, s66\n"
    "v_mfma_f32_16x16x32_bf16 v[40:43], v[72:75], v[88:91], v[40:43]\n"
    "ds_read_b64_tr_b16 v[108:109], v203 offset:8192\n"
    "s_cselect_b32 s63, 0, s63\n"
    "v_mfma_f32_16x16x32_bf16 v[44:47], v[72:75], v[92:95], v[44:47]\n"
    "ds_read_b64_tr_b16 v[110:111], v203 offset:9216\n"
    "s_waitcnt lgkmcnt(12)\n"
    "v_mfma_f32_16x16x32_bf16 v[60:63], v[76:79], v[92:95], v[60:63]\n"
    "v_mfma_f32_16x16x32_bf16 v[56:59], v[76:79], v[88:91], v[56:59]\n"
    "v_add_u32 v200, s64, v200\n"
    "v_add_u32 v201, s64, v201\n"
    "v_mfma_f32_16x16x32_bf16 v[52:55], v[76:79], v[84:87], v[52:55]\n"
    "v_add_u32 v202, s64, v202\n"
    "v_add_u32 v203, s64, v203\n"
    "v_mfma_f32_16x16x32_bf16 v[48:51], v[76:79], v[80:83], v[48:51]\n"
    "v_add_u32 v204, s64, v204\n"
    "v_add_u32 v205, s64, v205\n"
    "s_waitcnt lgkmcnt(0)\n"
    "v_mfma_f32_16x16x32_bf16 v[0:3], v[96:99], v[112:115], v[0:3]\n"
    "s_barrier\n"
    "ds_read_b64 v[128:129], v206 offset:0\n"
    "ds_read_b64 v[130:131], v252 offset:0\n"
    "v_mfma_f32_16x16x32_bf16 v[4:7], v[96:99], v[116:119], v[4:7]\n"
    "ds_read_b64 v[136:137], v206 offset:4096\n"
    "ds_read_b64 v[138:139], v252 offset:4096\n"
    "v_mfma_f32_16x16x32_bf16 v[8:11], v[96:99], v[120:123], v[8:11]\n"
    "ds_read_b64 v[144:145], v206 offset:8192\n"
    "ds_read_b64 v[146:147], v252 offset:8192\n"
    "v_mfma_f32_16x16x32_bf16 v[12:15], v[96:99], v[124:127], v[12:15]\n"
    "ds_read_b64 v[152:153], v206 offset:12288\n"
    "ds_read_b64 v[154:155], v252 offset:12288\n"
    "v_mfma_f32_16x16x32_bf16 v[28:31], v[100:103], v[124:127], v[28:31]\n"
    "ds_read_b64 v[132:133], v253 offset:0\n"
    "ds_read_b64 v[134:135], v254 offset:0\n"
    "v_mfma_f32_16x16x32_bf16 v[24:27], v[100:103], v[120:123], v[24:27]\n"
    "ds_read_b64 v[140:141], v253 offset:4096\n"
    "ds_read_b64 v[142:143], v254 offset:4096\n"
    "v_mfma_f32_16x16x32_bf16 v[20:23], v[100:103], v[116:119], v[20:23]\n"
    "s_waitcnt lgkmcnt(4)\n"
    "ds_read_b64 v[148:149], v253 offset:8192\n"
    "ds_read_b64 v[150:151], v254 offset:8192\n"
    "v_mfma_f32_16x16x32_bf16 v[16:19], v[100:103], v[112:115], v[16:19]\n"
    "ds_read_b64 v[156:157], v253 offset:12288\n"
    "ds_read_b64 v[158:159], v254 offset:12288\n"
    "v_mfma_f32_16x16x32_bf16 v[32:35], v[104:107], v[112:115], v[32:35]\n"
    "ds_read_b64_tr_b16 v[64:65], v200 offset:0\n"
    "ds_read_b64_tr_b16 v[66:67], v200 offset:1024\n"
    "v_mfma_f32_16x16x32_bf16 v[36:39], v[104:107], v[116:119], v[36:39]\n"
    "ds_read_b128 v[80:83], v204 offset:0\n"
    "ds_read_b128 v[84:87], v204 offset:2048\n"
    "v_mfma_f32_16x16x32_bf16 v[40:43], v[104:107], v[120:123], v[40:43]\n"
    "s_waitcnt lgkmcnt(4)\n"
    "ds_read_b128 v[88:91], v204 offset:4096\n"
    "ds_read_b128 v[92:95], v204 offset:6144\n"
    "v_mfma_f32_16x16x32_bf16 v[44:47], v[104:107], v[124:127], v[44:47]\n"
    "ds_read_b64_tr_b16 v[68:69], v201 offset:0\n"
    "ds_read_b64_tr_b16 v[70:71], v201 offset:1024\n"
    "v_mfma_f32_16x16x32_bf16 v[60:63], v[108:111], v[124:127], v[60:63]\n"
    "ds_read_b64_tr_b16 v[72:73], v202 offset:0\n"
    "ds_read_b64_tr_b16 v[74:75], v202 offset:1024\n"
    "v_mfma_f32_16x16x32_bf16 v[56:59], v[108:111], v[120:123], v[56:59]\n"
    "ds_read_b64_tr_b16 v[76:77], v203 offset:0\n"
    "ds_read_b64_tr_b16 v[78:79], v203 offset:1024\n"
    "v_mfma_f32_16x16x32_bf16 v[52:55], v[108:111], v[116:119], v[52:55]\n"
    "v_mfma_f32_16x16x32_bf16 v[48:51], v[108:111], v[112:115], v[48:51]\n"
    "1:\n"
    "s_waitcnt lgkmcnt(6)\n"
    "v_mfma_f32_16x16x32_bf16 v[0:3], v[64:67], v[80:83], v[0:3]\n"
    "ds_read_b64_tr_b16 v[96:97], v200 offset:8192\n"
    "v_mfma_f32_16x16x32_bf16 v[4:7], v[64:67], v[84:87], v[4:7]\n"
    "ds_read_b64_tr_b16 v[98:99], v200 offset:9216\n"
    "v_mfma_f32_16x16x32_bf16 v[8:11], v[64:67], v[88:91], v[8:11]\n"
    "ds_read_b128 v[112:115], v205 offset:0\n"
    "v_mfma_f32_16x16x32_bf16 v[12:15], v[64:67], v[92:95], v[12:15]\n"
    "ds_read_b128 v[116:119], v205 offset:2048\n"
    "s_waitcnt lgkmcnt(8)\n"
    "v_mfma_f32_16x16x32_bf16 v[28:31], v[68:71], v[92:95], v[28:31]\n"
    "ds_read_b128 v[120:123], v205 offset:4096\n"
    "v_mfma_f32_16x16x32_bf16 v[24:27], v[68:71], v[88:91], v[24:27]\n"
    "ds_read_b128 v[124:127], v205 offset:6144\n"
    "s_add_u32 s63, s63, 1\n"
    "v_mfma_f32_16x16x32_bf16 v[20:23], v[68:71], v[84:87], v[20:23]\n"
    "ds_read_b64_tr_b16 v[100:101], v201 offset:8192\n"
    "s_cmp_eq_u32 s63, 5\n"
    "v_mfma_f32_16x16x32_bf16 v[16:19], v[68:71], v[80:83], v[16:19]\n"
    "ds_read_b64_tr_b16 v[102:103], v201 offset:9216\n"
    "s_waitcnt lgkmcnt(10)\n"
    "v_mfma_f32_16x16x32_bf16 v[32:35], v[72:75], v[80:83], v[32:35]\n"
    "ds_read_b64_tr_b16 v[104:105], v202 offset:8192\n"
    "v_mfma_f32_16x16x32_bf16 v[36:39], v[72:75], v[84:87], v[36:39]\n"
    "ds_read_b64_tr_b16 v[106:107], v202 offset:9216\n"
    "s_cselect_b32 s64, 0xfffe0000, s66\n"
    "v_mfma_f32_16x16x32_bf16 v[40:43], v[72:75], v[88:91], v[40:43]\n"
    "ds_read_b64_tr_b16 v[108:109], v203 offset:8192\n"
    "s_cselect_b32 s63, 0, s63\n"
    "v_mfma_f32_16x16x32_bf16 v[44:47], v[72:75], v[92:95], v[44:47]\n"
    "ds_read_b64_tr_b16 v[110:111], v203 offset:9216\n"
    "s_waitcnt lgkmcnt(12)\n"
    "v_mfma_f32_16x16x32_bf16 v[60:63], v[76:79], v[92:95], v[60:63]\n"
    "v_mfma_f32_16x16x32_bf16 v[56:59], v[76:79], v[88:91], v[56:59]\n"
    "v_add_u32 v200, s64, v200\n"
    "v_add_u32 v201, s64, v201\n"
    "v_mfma_f32_16x16x32_bf16 v[52:55], v[76:79], v[84:87], v[52:55]\n"
    "v_add_u32 v202, s64, v202\n"
    "v_add_u32 v203, s64, v203\n"
    "v_mfma_f32_16x16x32_bf16 v[48:51], v[76:79], v[80:83], v[48:51]\n"
    "v_add_u32 v204, s64, v204\n"
    "v_add_u32 v205, s64, v205\n"
    "s_waitcnt lgkmcnt(6)\n"
    "v_mfma_f32_16x16x32_bf16 v[0:3], v[96:99], v[112:115], v[0:3]\n"
    "s_barrier\n"
    "ds_read_b64_tr_b16 v[64:65], v200 offset:0\n"
    "v_mfma_f32_16x16x32_bf16 v[4:7], v[96:99], v[116:119], v[4:7]\n"
    "ds_read_b64_tr_b16 v[66:67], v200 offset:1024\n"
    "v_mfma_f32_16x16x32_bf16 v[8:11], v[96:99], v[120:123], v[8:11]\n"
    "ds_read_b128 v[80:83], v204 offset:0\n"
    "v_mfma_f32_16x16x32_bf16 v[12:15], v[96:99], v[124:127], v[12:15]\n"
    "ds_read_b128 v[84:87], v204 offset:2048\n"
    "s_waitcnt lgkmcnt(8)\n"
    "v_mfma_f32_16x16x32_bf16 v[28:31], v[100:103], v[124:127], v[28:31]\n"
    "ds_read_b128 v[88:91], v204 offset:4096\n"
    "v_mfma_f32_16x16x32_bf16 v[24:27], v[100:103], v[120:123], v[24:27]\n"
    "ds_read_b128 v[92:95], v204 offset:6144\n"
    "v_mfma_f32_16x16x32_bf16 v[20:23], v[100:103], v[116:119], v[20:23]\n"
    "ds_read_b64_tr_b16 v[68:69], v201 offset:0\n"
    "v_mfma_f32_16x16x32_bf16 v[16:19], v[100:103], v[112:115], v[16:19]\n"
    "ds_read_b64_tr_b16 v[70:71], v201 offset:1024\n"
    "s_waitcnt lgkmcnt(10)\n"
    "v_mfma_f32_16x16x32_bf16 v[32:35], v[104:107], v[112:115], v[32:35]\n"
    "ds_read_b64_tr_b16 v[72:73], v202 offset:0\n"
    "v_mfma_f32_16x16x32_bf16 v[36:39], v[104:107], v[116:119], v[36:39]\n"
    "ds_read_b64_tr_b16 v[74:75], v202 offset:1024\n"
    "v_mfma_f32_16x16x32_bf16 v[40:43], v[104:107], v[120:123], v[40:43]\n"
    "ds_read_b64_tr_b16 v[76:77], v203 offset:0\n"
    "v_mfma_f32_16x16x32_bf16 v[44:47], v[104:107], v[124:127], v[44:47]\n"
    "ds_read_b64_tr_b16 v[78:79], v203 offset:1024\n"
    "s_waitcnt lgkmcnt(12)\n"
    "v_mfma_f32_16x16x32_bf16 v[60:63], v[108:111], v[124:127], v[60:63]\n"
    "v_mfma_f32_16x16x32_bf16 v[56:59], v[108:111], v[120:123], v[56:59]\n"
    "s_sub_u32 s62, s62, 1\n"
    "v_mfma_f32_16x16x32_bf16 v[52:55], v[108:111], v[116:119], v[52:55]\n"
    "s_cmp_lg_u32 s62, 0\n"
    "v_mfma_f32_16x16x32_bf16 v[48:51], v[108:111], v[112:115], v[48:51]\n"
    "s_cbranch_scc1 1b\n"
    "s_waitcnt lgkmcnt(6)\n"
    "v_mfma_f32_16x16x32_bf16 v[0:3], v[64:67], v[80:83], v[0:3]\n"
    "ds_read_b64_tr_b16 v[96:97], v200 offset:8192\n"
    "v_mfma_f32_16x16x32_bf16 v[4:7], v[64:67], v[84:87], v[4:7]\n"
    "ds_read_b64_tr_b16 v[98:99], v200 offset:9216\n"
    "v_mfma_f32_16x16x32_bf16 v[8:11], v[64:67], v[88:91], v[8:11]\n"
    "ds_read_b128 v[112:115], v205 offset:0\n"
    "v_mfma_f32_16x16x32_bf16 v[12:15], v[64:67], v[92:95], v[12:15]\n"
    "ds_read_b128 v[116:119], v205 offset:2048\n"
    "s_waitcnt lgkmcnt(8)\n"
    "v_mfma_f32_16x16x32_bf16 v[28:31], v[68:71], v[92:95], v[28:31]\n"
    "ds_read_b128 v[120:123], v205 offset:4096\n"
    "v_mfma_f32_16x16x32_bf16 v[24:27], v[68:71], v[88:91], v[24:27]\n"
    "ds_read_b128 v[124:127], v205 offset:6144\n"
    "v_mfma_f32_16x16x32_bf16 v[20:23], v[68:71], v[84:87], v[20:23]\n"
    "ds_read_b64_tr_b16 v[100:101], v201 offset:8192\n"
    "v_mfma_f32_16x16x32_bf16 v[16:19], v[68:71], v[80:83], v[16:19]\n"
    "ds_read_b64_tr_b16 v[102:103], v201 offset:9216\n"
    "s_waitcnt lgkmcnt(10)\n"
    "v_mfma_f32_16x16x32_bf16 v[32:35], v[72:75], v[80:83], v[32:35]\n"
    "ds_read_b64_tr_b16 v[104:105], v202 offset:8192\n"
    "v_mfma_f32_16x16x32_bf16 v[36:39], v[72:75], v[84:87], v[36:39]\n"
    "ds_read_b64_tr_b16 v[106:107], v202 offset:9216\n"
    "v_mfma_f32_16x16x32_bf16 v[40:43], v[72:75], v[88:91], v[40:43]\n"
    "ds_read_b64_tr_b16 v[108:109], v203 offset:8192\n"
    "v_mfma_f32_16x16x32_bf16 v[44:47], v[72:75], v[92:95], v[44:47]\n"
    "ds_read_b64_tr_b16 v[110:111], v203 offset:9216\n"
    "s_waitcnt lgkmcnt(12)\n"
    "v_mfma_f32_16x16x32_bf16 v[60:63], v[76:79], v[92:95], v[60:63]\n"
    "v_mfma_f32_16x16x32_bf16 v[56:59], v[76:79], v[88:91], v[56:59]\n"
    "v_mfma_f32_16x16x32_bf16 v[52:55], v[76:79], v[84:87], v[52:55]\n"
    "v_mfma_f32_16x16x32_bf16 v[48:51], v[76:79], v[80:83], v[48:51]\n"
    "s_waitcnt lgkmcnt(6)\n"
    "v_mfma_f32_16x16x32_bf16 v[0:3], v[96:99], v[112:115], v[0:3]\n"
    "s_barrier\n"
    "v_mfma_f32_16x16x32_bf16 v[4:7], v[96:99], v[116:119], v[4:7]\n"
    "v_mfma_f32_16x16x32_bf16 v[8:11], v[96:99], v[120:123], v[8:11]\n"
    "v_mfma_f32_16x16x32_bf16 v[12:15], v[96:99], v[124:127], v[12:15]\n"
    "s_waitcnt lgkmcnt(4)\n"
    "v_mfma_f32_16x16x32_bf16 v[28:31], v[100:103], v[124:127], v[28:31]\n"
    "v_mfma_f32_16x16x32_bf16 v[24:27], v[100:103], v[120:123], v[24:27]\n"
    "v_mfma_f32_16x16x32_bf16 v[20:23], v[100:103], v[116:119], v[20:23]\n"
    "v_cvt_pk_bf16_f32 v160, v0, v1\n"
    "v_cvt_pk_bf16_f32 v161, v2, v3\n"
    "v_mfma_f32_16x16x32_bf16 v[16:19], v[100:103], v[112:115], v[16:19]\n"
    "v_cvt_pk_bf16_f32 v168, v4, v5\n"
    "v_cvt_pk_bf16_f32 v169, v6, v7\n"
    "s_waitcnt lgkmcnt(2)\n"
    "v_mfma_f32_16x16x32_bf16 v[32:35], v[104:107], v[112:115], v[32:35]\n"
    "v_cvt_pk_bf16_f32 v176, v8, v9\n"
    "v_cvt_pk_bf16_f32 v177, v10, v11\n"
    "v_mfma_f32_16x16x32_bf16 v[36:39], v[104:107], v[116:119], v[36:39]\n"
    "v_cvt_pk_bf16_f32 v184, v12, v13\n"
    "v_cvt_pk_bf16_f32 v185, v14, v15\n"
    "v_mfma_f32_16x16x32_bf16 v[40:43], v[104:107], v[120:123], v[40:43]\n"
    "v_cvt_pk_bf16_f32 v186, v28, v29\n"
    "v_cvt_pk_bf16_f32 v187, v30, v31\n"
    "v_mfma_f32_16x16x32_bf16 v[44:47], v[104:107], v[124:127], v[44:47]\n"
    "v_cvt_pk_bf16_f32 v178, v24, v25\n"
    "v_cvt_pk_bf16_f32 v179, v26, v27\n"
    "s_waitcnt lgkmcnt(0)\n"
    "v_mfma_f32_16x16x32_bf16 v[60:63], v[108:111], v[124:127], v[60:63]\n"
    "v_cvt_pk_bf16_f32 v170, v20, v21\n"
    "v_cvt_pk_bf16_f32 v171, v22, v23\n"
    "v_mfma_f32_16x16x32_bf16 v[56:59], v[108:111], v[120:123], v[56:59]\n"
    "v_cvt_pk_bf16_f32 v162, v16, v17\n"
    "v_cvt_pk_bf16_f32 v163, v18, v19\n"
    "v_mfma_f32_16x16x32_bf16 v[52:55], v[108:111], v[116:119], v[52:55]\n"
    "v_cvt_pk_bf16_f32 v164, v32, v33\n"
    "v_cvt_pk_bf16_f32 v165, v34, v35\n"
    "v_mfma_f32_16x16x32_bf16 v[48:51], v[108:111], v[112:115], v[48:51]\n"
    "v_cvt_pk_bf16_f32 v172, v36, v37\n"
    "v_cvt_pk_bf16_f32 v173, v38, v39\n"
    "v_mfma_f32_16x16x32_bf16 v[88:91], v[144:147], v[184:187], 0\n"
    "v_mfma_f32_16x16x32_bf16 v[92:95], v[152:155], v[184:187], 0\n"
    "v_cvt_pk_bf16_f32 v180, v40, v41\n"
    "v_cvt_pk_bf16_f32 v181, v42, v43\n"
    "v_mfma_f32_16x16x32_bf16 v[80:83], v[144:147], v[176:179], 0\n"
    "v_mfma_f32_16x16x32_bf16 v[84:87], v[152:155], v[176:179], 0\n"
    "v_cvt_pk_bf16_f32 v188, v44, v45\n"
    "v_cvt_pk_bf16_f32 v189, v46, v47\n"
    "v_mfma_f32_16x16x32_bf16 v[72:75], v[128:131], v[168:171], 0\n"
    "v_mfma_f32_16x16x32_bf16 v[76:79], v[136:139], v[168:171], 0\n"
    "v_cvt_pk_bf16_f32 v190, v60, v61\n"
    "v_cvt_pk_bf16_f32 v191, v62, v63\n"
    "v_mfma_f32_16x16x32_bf16 v[64:67], v[128:131], v[160:163], 0\n"
    "v_mfma_f32_16x16x32_bf16 v[68:71], v[136:139], v[160:163], 0\n"
    "v_cvt_pk_bf16_f32 v182, v56, v57\n"
    "v_cvt_pk_bf16_f32 v183, v58, v59\n"
    "v_mfma_f32_16x16x32_bf16 v[88:91], v[148:151], v[188:191], v[88:91]\n"
    "v_mfma_f32_16x16x32_bf16 v[92:95], v[156:159], v[188:191], v[92:95]\n"
    "v_cvt_pk_bf16_f32 v174, v52, v53\n"
    "v_cvt_pk_bf16_f32 v175, v54, v55\n"
    "v_mfma_f32_16x16x32_bf16 v[80:83], v[148:151], v[180:183], v[80:83]\n"
    "v_mfma_f32_16x16x32_bf16 v[84:87], v[156:159], v[180:183], v[84:87]\n"
    "v_cvt_pk_bf16_f32 v166, v48, v49\n"
    "v_cvt_pk_bf16_f32 v167, v50, v51\n"
    "v_mfma_f32_16x16x32_bf16 v[72:75], v[132:135], v[172:175], v[72:75]\n"
    "v_mfma_f32_16x16x32_bf16 v[76:79], v[140:143], v[172:175], v[76:79]\n"
    "v_mfma_f32_16x16x32_bf16 v[64:67], v[132:135], v[164:167], v[64:67]\n"
    "v_mfma_f32_16x16x32_bf16 v[68:71], v[140:143], v[164:167], v[68:71]\n"
    "v_xor_b32 v219, 0x1000, v207\n"
    "s_waitcnt vmcnt(0)\n"
    "v_add_u32 v214, 0x7f4a7c15, v214\n"
    "v_add_u32 v215, 0x9e3779b9, v215\n"
    "v_xor_b32 v200, 0x20, v210\n"
    "v_xor_b32 v201, 0x40, v210\n"
    "v_xor_b32 v202, 0x60, v210\n"
    "s_cmp_eq_u32 s61, 0\n"
    "s_cbranch_scc1 2f\n"
    "ds_write_b128 v207, v[64:67] offset:0\n"
    "ds_write_b128 v207, v[68:71] offset:1024\n"
    "ds_write_b128 v207, v[72:75] offset:2048\n"
    "ds_write_b128 v207, v[76:79] offset:3072\n"
    "s_branch 3f\n"
    "2:\n"
    "ds_write_b128 v207, v[80:83] offset:0\n"
    "ds_write_b128 v207, v[84:87] offset:1024\n"
    "ds_write_b128 v207, v[88:91] offset:2048\n"
    "ds_write_b128 v207, v[92:95] offset:3072\n"
    "3:\n"
    "v_add_u32 v203, 0x10000, v210\n"
    "v_add_u32 v204, 0x10000, v200\n"
    "v_add_u32 v205, 0x10000, v201\n"
    "v_add_u32 v206, 0x10000, v202\n"
    "s_waitcnt lgkmcnt(0)\n"
    "s_barrier\n"
    "ds_read_b128 v[96:99], v219 offset:0\n"
    "ds_read_b128 v[100:103], v219 offset:1024\n"
    "ds_read_b128 v[104:107], v219 offset:2048\n"
    "ds_read_b128 v[108:111], v219 offset:3072\n"
    "s_waitcnt lgkmcnt(0)\n"
    "s_cmp_eq_u32 s61, 0\n"
    "s_cbranch_scc1 4f\n"
    "v_pk_add_f32 v[112:113], v[80:81], v[96:97]\n"
    "v_pk_add_f32 v[114:115], v[82:83], v[98:99]\n"
    "v_pk_add_f32 v[116:117], v[84:85], v[100:101]\n"
    "v_pk_add_f32 v[118:119], v[86:87], v[102:103]\n"
    "v_pk_add_f32 v[120:121], v[88:89], v[104:105]\n"
    "v_pk_add_f32 v[122:123], v[90:91], v[106:107]\n"
    "v_pk_add_f32 v[124:125], v[92:93], v[108:109]\n"
    "v_pk_add_f32 v[126:127], v[94:95], v[110:111]\n"
    "s_branch 5f\n"
    "4:\n"
    "v_pk_add_f32 v[112:113], v[64:65], v[96:97]\n"
    "v_pk_add_f32 v[114:115], v[66:67], v[98:99]\n"
    "v_pk_add_f32 v[116:117], v[68:69], v[100:101]\n"
    "v_pk_add_f32 v[118:119], v[70:71], v[102:103]\n"
    "v_pk_add_f32 v[120:121], v[72:73], v[104:105]\n"
    "v_pk_add_f32 v[122:123], v[74:75], v[106:107]\n"
    "v_pk_add_f32 v[124:125], v[76:77], v[108:109]\n"
    "v_pk_add_f32 v[126:127], v[78:79], v[110:111]\n"
    "5:\n"
    "v_max3_f32 v96, v112, v113, v114\n"
    "v_max3_f32 v97, v120, v121, v122\n"
    "v_max3_f32 v96, v96, v115, v116\n"
    "v_max3_f32 v97, v97, v123, v124\n"
    "v_max3_f32 v96, v96, v117, v118\n"
    "v_max3_f32 v97, v97, v125, v126\n"
    "v_max_f32 v96, v96, v119\n"
    "v_max_f32 v97, v97, v127\n"
    "s_nop 1\n"
    "v_permlane32_swap_b32 v96, v97\n"
    "v_max_f32 v96, v96, v97\n"
    "v_mov_b32 v97, v96\n"
    "s_nop 1\n"
    "v_permlane16_swap_b32 v97, v96\n"
    "v_max_f32 v97, v96, v97\n"
    "v_mul_f32 v97, s65, v97\n"
    "v_mov_b32 v96, v97\n"
    "s_nop 1\n"
    "v_permlane32_swap_b32 v96, v97\n"
    "v_pk_fma_f32 v[112:113], v[112:113], s[64:65], v[96:97] op_sel:[0,1,0] op_sel_hi:[1,1,0] neg_lo:[0,0,1] neg_hi:[0,0,1]\n"
    "v_pk_fma_f32 v[114:115], v[114:115], s[64:65], v[96:97] op_sel:[0,1,0] op_sel_hi:[1,1,0] neg_lo:[0,0,1] neg_hi:[0,0,1]\n"
    "v_pk_fma_f32 v[116:117], v[116:117], s[64:65], v[96:97] op_sel:[0,1,0] op_sel_hi:[1,1,0] neg_lo:[0,0,1] neg_hi:[0,0,1]\n"
    "v_pk_fma_f32 v[118:119], v[118:119], s[64:65], v[96:97] op_sel:[0,1,0] op_sel_hi:[1,1,0] neg_lo:[0,0,1] neg_hi:[0,0,1]\n"
    "v_pk_fma_f32 v[120:121], v[120:121], s[64:65], v[96:97] op_sel:[0,1,1] op_sel_hi:[1,1,1] neg_lo:[0,0,1] neg_hi:[0,0,1]\n"
    "v_pk_fma_f32 v[122:123], v[122:123], s[64:65], v[96:97] op_sel:[0,1,1] op_sel_hi:[1,1,1] neg_lo:[0,0,1] neg_hi:[0,0,1]\n"
    "v_pk_fma_f32 v[124:125], v[124:125], s[64:65], v[96:97] op_sel:[0,1,1] op_sel_hi:[1,1,1] neg_lo:[0,0,1] neg_hi:[0,0,1]\n"
    "v_pk_fma_f32 v[126:127], v[126:127], s[64:65], v[96:97] op_sel:[0,1,1] op_sel_hi:[1,1,1] neg_lo:[0,0,1] neg_hi:[0,0,1]\n"
    "v_exp_f32 v112, v112\n"
    "v_exp_f32 v120, v120\n"
    "v_exp_f32 v113, v113\n"
    "v_exp_f32 v121, v121\n"
    "v_exp_f32 v114, v114\n"
    "v_exp_f32 v122, v122\n"
    "v_exp_f32 v115, v115\n"
    "v_exp_f32 v123, v123\n"
    "v_exp_f32 v116, v116\n"
    "v_exp_f32 v124, v124\n"
    "v_pk_add_f32 v[100:101], v[112:113], v[114:115]\n"
    "v_pk_add_f32 v[102:103], v[120:121], v[122:123]\n"
    "v_exp_f32 v117, v117\n"
    "v_exp_f32 v125, v125\n"
    "v_exp_f32 v118, v118\n"
    "v_exp_f32 v126, v126\n"
    "v_pk_add_f32 v[100:101], v[100:101], v[116:117]\n"
    "v_pk_add_f32 v[102:103], v[102:103], v[124:125]\n"
    "v_exp_f32 v119, v119\n"
    "v_exp_f32 v127, v127\n"
    "v_pk_add_f32 v[100:101], v[100:101], v[118:119]\n"
    "v_pk_add_f32 v[102:103], v[102:103], v[126:127]\n"
    "v_add_f32 v98, v100, v101\n"
    "v_add_f32 v99, v102, v103\n"
    "s_nop 1\n"
    "v_permlane32_swap_b32 v98, v99\n"
    "v_add_f32 v98, v98, v99\n"
    "v_mov_b32 v99, v98\n"
    "s_nop 1\n"
    "v_permlane16_swap_b32 v99, v98\n"
    "v_add_f32 v99, v98, v99\n"
    "v_rcp_f32 v99, v99\n"
    "s_nop 0\n"
    "v_mov_b32 v98, v99\n"
    "s_nop 1\n"
    "v_permlane32_swap_b32 v98, v99\n"
    "v_pk_mul_f32 v[112:113], v[112:113], v[98:99] op_sel:[0,0] op_sel_hi:[1,0]\n"
    "v_cvt_pk_bf16_f32 v104, v112, v113\n"
    "v_pk_mul_f32 v[114:115], v[114:115], v[98:99] op_sel:[0,0] op_sel_hi:[1,0]\n"
    "v_cvt_pk_bf16_f32 v105, v114, v115\n"
    "v_pk_mul_f32 v[116:117], v[116:117], v[98:99] op_sel:[0,0] op_sel_hi:[1,0]\n"
    "v_cvt_pk_bf16_f32 v106, v116, v117\n"
    "v_pk_mul_f32 v[118:119], v[118:119], v[98:99] op_sel:[0,0] op_sel_hi:[1,0]\n"
    "v_cvt_pk_bf16_f32 v107, v118, v119\n"
    "buffer_store_dwordx4 v[104:107], v208, s[28:31], 0 offen sc1\n"
    "v_pk_mul_f32 v[120:121], v[120:121], v[98:99] op_sel:[0,1] op_sel_hi:[1,1]\n"
    "v_cvt_pk_bf16_f32 v108, v120, v121\n"
    "v_pk_mul_f32 v[122:123], v[122:123], v[98:99] op_sel:[0,1] op_sel_hi:[1,1]\n"
    "v_cvt_pk_bf16_f32 v109, v122, v123\n"
    "v_pk_mul_f32 v[124:125], v[124:125], v[98:99] op_sel:[0,1] op_sel_hi:[1,1]\n"
    "v_cvt_pk_bf16_f32 v110, v124, v125\n"
    "v_pk_mul_f32 v[126:127], v[126:127], v[98:99] op_sel:[0,1] op_sel_hi:[1,1]\n"
    "v_cvt_pk_bf16_f32 v111, v126, v127\n"
    "buffer_store_dwordx4 v[108:111], v208, s[28:31], 0 offen offset:1024 sc1\n"
    "s_barrier\n"
    "v_add_u32 v207, 0x10000, v212\n"
    "v_add_u32 v219, 0x20000, v212\n"
    "s_cmp_eq_u32 s69, 0\n"
    "s_mov_b32 s67, 0x14000\n"
    "s_cselect_b32 s67, 0xc000, s67\n"
    "v_xor_b32 v252, 0x0, v211\n"
    "v_add_u32 v252, s67, v252\n"
    "ds_read_b128 v[160:163], v252 offset:0\n"
    "ds_read_b128 v[164:167], v252 offset:4096\n"
    "ds_read_b64_tr_b16 v[128:129], v210 offset:0\n"
    "ds_read_b64_tr_b16 v[130:131], v210 offset:512\n"
    "ds_read_b64_tr_b16 v[132:133], v200 offset:0\n"
    "ds_read_b64_tr_b16 v[134:135], v200 offset:512\n"
    "v_xor_b32 v253, 0x40, v211\n"
    "v_add_u32 v253, s67, v253\n"
    "ds_read_b128 v[168:171], v253 offset:0\n"
    "ds_read_b128 v[172:175], v253 offset:4096\n"
    "ds_read_b64_tr_b16 v[136:137], v210 offset:4096\n"
    "ds_read_b64_tr_b16 v[138:139], v210 offset:4608\n"
    "ds_read_b64_tr_b16 v[140:141], v200 offset:4096\n"
    "ds_read_b64_tr_b16 v[142:143], v200 offset:4608\n"
    "v_xor_b32 v252, 0x80, v211\n"
    "v_add_u32 v252, s67, v252\n"
    "ds_read_b128 v[176:179], v252 offset:0\n"
    "ds_read_b128 v[180:183], v252 offset:4096\n"
    "v_xor_b32 v253, 0xc0, v211\n"
    "v_add_u32 v253, s67, v253\n"
    "ds_read_b128 v[184:187], v253 offset:0\n"
    "s_waitcnt lgkmcnt(14)\n"
    "ds_read_b128 v[188:191], v253 offset:4096\n"
    "s_waitcnt lgkmcnt(10)\n"
    "v_mfma_f32_16x16x32_bf16 v[32:35], v[160:163], v[128:131], 0\n"
    "ds_read_b64_tr_b16 v[144:145], v210 offset:8192\n"
    "v_mfma_f32_16x16x32_bf16 v[36:39], v[164:167], v[128:131], 0\n"
    "ds_read_b64_tr_b16 v[146:147], v210 offset:8704\n"
    "v_mfma_f32_16x16x32_bf16 v[44:47], v[164:167], v[132:135], 0\n"
    "ds_read_b64_tr_b16 v[148:149], v200 offset:8192\n"
    "v_mfma_f32_16x16x32_bf16 v[40:43], v[160:163], v[132:135], 0\n"
    "ds_read_b64_tr_b16 v[150:151], v200 offset:8704\n"
    "s_waitcnt lgkmcnt(8)\n"
    "v_mfma_f32_16x16x32_bf16 v[32:35], v[168:171], v[136:139], v[32:35]\n"
    "ds_read_b64_tr_b16 v[152:153], v210 offset:12288\n"
    "v_mfma_f32_16x16x32_bf16 v[36:39], v[172:175], v[136:139], v[36:39]\n"
    "ds_read_b64_tr_b16 v[154:155], v210 offset:12800\n"
    "v_mfma_f32_16x16x32_bf16 v[44:47], v[172:175], v[140:143], v[44:47]\n"
    "ds_read_b64_tr_b16 v[156:157], v200 offset:12288\n"
    "v_mfma_f32_16x16x32_bf16 v[40:43], v[168:171], v[140:143], v[40:43]\n"
    "ds_read_b64_tr_b16 v[158:159], v200 offset:12800\n"
    "s_waitcnt lgkmcnt(4)\n"
    "v_mfma_f32_16x16x32_bf16 v[32:35], v[176:179], v[144:147], v[32:35]\n"
    "ds_read_b64_tr_b16 v[128:129], v201 offset:0\n"
    "v_mfma_f32_16x16x32_bf16 v[36:39], v[180:183], v[144:147], v[36:39]\n"
    "ds_read_b64_tr_b16 v[130:131], v201 offset:512\n"
    "v_mfma_f32_16x16x32_bf16 v[44:47], v[180:183], v[148:151], v[44:47]\n"
    "ds_read_b64_tr_b16 v[132:133], v202 offset:0\n"
    "v_mfma_f32_16x16x32_bf16 v[40:43], v[176:179], v[148:151], v[40:43]\n"
    "ds_read_b64_tr_b16 v[134:135], v202 offset:512\n"
    "s_waitcnt lgkmcnt(4)\n"
    "v_mfma_f32_16x16x32_bf16 v[32:35], v[184:187], v[152:155], v[32:35]\n"
    "ds_read_b64_tr_b16 v[136:137], v201 offset:4096\n"
    "v_mfma_f32_16x16x32_bf16 v[36:39], v[188:191], v[152:155], v[36:39]\n"
    "ds_read_b64_tr_b16 v[138:139], v201 offset:4608\n"
    "v_mfma_f32_16x16x32_bf16 v[44:47], v[188:191], v[156:159], v[44:47]\n"
    "ds_read_b64_tr_b16 v[140:141], v202 offset:4096\n"
    "v_mfma_f32_16x16x32_bf16 v[40:43], v[184:187], v[156:159], v[40:43]\n"
    "ds_read_b64_tr_b16 v[142:143], v202 offset:4608\n"
    "s_waitcnt lgkmcnt(4)\n"
    "v_mfma_f32_16x16x32_bf16 v[48:51], v[160:163], v[128:131], 0\n"
    "ds_read_b64_tr_b16 v[144:145], v201 offset:8192\n"
    "v_mfma_f32_16x16x32_bf16 v[52:55], v[164:167], v[128:131], 0\n"
    "ds_read_b64_tr_b16 v[146:147], v201 offset:8704\n"
    "v_mfma_f32_16x16x32_bf16 v[60:63], v[164:167], v[132:135], 0\n"
    "ds_read_b64_tr_b16 v[148:149], v202 offset:8192\n"
    "v_cvt_pk_bf16_f32 v64, v32, v33\n"
    "v_mfma_f32_16x16x32_bf16 v[56:59], v[160:163], v[132:135], 0\n"
    "ds_read_b64_tr_b16 v[150:151], v202 offset:8704\n"
    "v_cvt_pk_bf16_f32 v65, v34, v35\n"
    "s_waitcnt lgkmcnt(4)\n"
    "v_mfma_f32_16x16x32_bf16 v[48:51], v[168:171], v[136:139], v[48:51]\n"
    "ds_read_b64_tr_b16 v[152:153], v201 offset:12288\n"
    "v_cvt_pk_bf16_f32 v66, v36, v37\n"
    "v_mfma_f32_16x16x32_bf16 v[52:55], v[172:175], v[136:139], v[52:55]\n"
    "ds_read_b64_tr_b16 v[154:155], v201 offset:12800\n"
    "v_cvt_pk_bf16_f32 v67, v38, v39\n"
    "v_mfma_f32_16x16x32_bf16 v[60:63], v[172:175], v[140:143], v[60:63]\n"
    "ds_read_b64_tr_b16 v[156:157], v202 offset:12288\n"
    "v_cvt_pk_bf16_f32 v70, v44, v45\n"
    "v_mfma_f32_16x16x32_bf16 v[56:59], v[168:171], v[140:143], v[56:59]\n"
    "ds_read_b64_tr_b16 v[158:159], v202 offset:12800\n"
    "v_cvt_pk_bf16_f32 v71, v46, v47\n"
    "s_waitcnt lgkmcnt(4)\n"
    "v_mfma_f32_16x16x32_bf16 v[48:51], v[176:179], v[144:147], v[48:51]\n"
    "s_waitcnt vmcnt(0)\n"
    "buffer_store_dwordx2 v[214:215], v209, s[28:31], 0 offen sc1\n"
    "s_barrier\n"
    "ds_read_b64_tr_b16 v[128:129], v210 offset:32768\n"
    "v_cvt_pk_bf16_f32 v68, v40, v41\n"
    "v_mfma_f32_16x16x32_bf16 v[52:55], v[180:183], v[144:147], v[52:55]\n"
    "ds_read_b64_tr_b16 v[130:131], v210 offset:33280\n"
    "v_cvt_pk_bf16_f32 v69, v42, v43\n"
    "v_mfma_f32_16x16x32_bf16 v[60:63], v[180:183], v[148:151], v[60:63]\n"
    "ds_read_b64_tr_b16 v[132:133], v200 offset:32768\n"
    "v_mfma_f32_16x16x32_bf16 v[56:59], v[176:179], v[148:151], v[56:59]\n"
    "ds_read_b64_tr_b16 v[134:135], v200 offset:33280\n"
    "s_waitcnt lgkmcnt(4)\n"
    "v_mfma_f32_16x16x32_bf16 v[48:51], v[184:187], v[152:155], v[48:51]\n"
    "ds_read_b64_tr_b16 v[136:137], v210 offset:36864\n"
    "v_mfma_f32_16x16x32_bf16 v[52:55], v[188:191], v[152:155], v[52:55]\n"
    "ds_read_b64_tr_b16 v[138:139], v210 offset:37376\n"
    "v_mfma_f32_16x16x32_bf16 v[60:63], v[188:191], v[156:159], v[60:63]\n"
    "ds_read_b64_tr_b16 v[140:141], v200 offset:36864\n"
    "v_mfma_f32_16x16x32_bf16 v[56:59], v[184:187], v[156:159], v[56:59]\n"
    "ds_read_b64_tr_b16 v[142:143], v200 offset:37376\n"
    "v_mov_b32 v208, s71\n"
    "s_waitcnt lgkmcnt(4)\n"
    "v_mfma_f32_16x16x32_bf16 v[32:35], v[160:163], v[128:131], 0\n"
    "ds_read_b64_tr_b16 v[144:145], v210 offset:40960\n"
    "v_mfma_f32_16x16x32_bf16 v[36:39], v[164:167], v[128:131], 0\n"
    "ds_read_b64_tr_b16 v[146:147], v210 offset:41472\n"
    "v_mfma_f32_16x16x32_bf16 v[44:47], v[164:167], v[132:135], 0\n"
    "ds_read_b64_tr_b16 v[148:149], v200 offset:40960\n"
    "v_cvt_pk_bf16_f32 v72, v48, v49\n"
    "v_mfma_f32_16x16x32_bf16 v[40:43], v[160:163], v[132:135], 0\n"
    "ds_read_b64_tr_b16 v[150:151], v200 offset:41472\n"
    "v_cvt_pk_bf16_f32 v73, v50, v51\n"
    "s_waitcnt lgkmcnt(4)\n"
    "v_mfma_f32_16x16x32_bf16 v[32:35], v[168:171], v[136:139], v[32:35]\n"
    "ds_read_b64_tr_b16 v[152:153], v210 offset:45056\n"
    "v_cvt_pk_bf16_f32 v74, v52, v53\n"
    "v_mfma_f32_16x16x32_bf16 v[36:39], v[172:175], v[136:139], v[36:39]\n"
    "ds_read_b64_tr_b16 v[154:155], v210 offset:45568\n"
    "v_cvt_pk_bf16_f32 v75, v54, v55\n"
    "v_mfma_f32_16x16x32_bf16 v[44:47], v[172:175], v[140:143], v[44:47]\n"
    "ds_read_b64_tr_b16 v[156:157], v200 offset:45056\n"
    "v_cvt_pk_bf16_f32 v78, v60, v61\n"
    "v_mfma_f32_16x16x32_bf16 v[40:43], v[168:171], v[140:143], v[40:43]\n"
    "ds_read_b64_tr_b16 v[158:159], v200 offset:45568\n"
    "v_cvt_pk_bf16_f32 v79, v62, v63\n"
    "s_waitcnt lgkmcnt(4)\n"
    "v_mfma_f32_16x16x32_bf16 v[32:35], v[176:179], v[144:147], v[32:35]\n"
    "ds_read_b64_tr_b16 v[128:129], v201 offset:32768\n"
    "v_cvt_pk_bf16_f32 v76, v56, v57\n"
    "v_mfma_f32_16x16x32_bf16 v[36:39], v[180:183], v[144:147], v[36:39]\n"
    "ds_read_b64_tr_b16 v[130:131], v201 offset:33280\n"
    "v_cvt_pk_bf16_f32 v77, v58, v59\n"
    "v_mfma_f32_16x16x32_bf16 v[44:47], v[180:183], v[148:151], v[44:47]\n"
    "ds_read_b64_tr_b16 v[132:133], v202 offset:32768\n"
    "v_mfma_f32_16x16x32_bf16 v[40:43], v[176:179], v[148:151], v[40:43]\n"
    "ds_read_b64_tr_b16 v[134:135], v202 offset:33280\n"
    "s_waitcnt lgkmcnt(4)\n"
    "v_mfma_f32_16x16x32_bf16 v[32:35], v[184:187], v[152:155], v[32:35]\n"
    "ds_read_b64_tr_b16 v[136:137], v201 offset:36864\n"
    "v_mfma_f32_16x16x32_bf16 v[36:39], v[188:191], v[152:155], v[36:39]\n"
    "ds_read_b64_tr_b16 v[138:139], v201 offset:37376\n"
    "v_mfma_f32_16x16x32_bf16 v[44:47], v[188:191], v[156:159], v[44:47]\n"
    "ds_read_b64_tr_b16 v[140:141], v202 offset:36864\n"
    "v_mfma_f32_16x16x32_bf16 v[40:43], v[184:187], v[156:159], v[40:43]\n"
    "ds_read_b64_tr_b16 v[142:143], v202 offset:37376\n"
    "s_waitcnt lgkmcnt(4)\n"
    "v_mfma_f32_16x16x32_bf16 v[48:51], v[160:163], v[128:131], 0\n"
    "ds_read_b64_tr_b16 v[144:145], v201 offset:40960\n"
    "v_mfma_f32_16x16x32_bf16 v[52:55], v[164:167], v[128:131], 0\n"
    "ds_read_b64_tr_b16 v[146:147], v201 offset:41472\n"
    "v_mfma_f32_16x16x32_bf16 v[60:63], v[164:167], v[132:135], 0\n"
    "ds_read_b64_tr_b16 v[148:149], v202 offset:40960\n"
    "v_cvt_pk_bf16_f32 v80, v32, v33\n"
    "v_mfma_f32_16x16x32_bf16 v[56:59], v[160:163], v[132:135], 0\n"
    "ds_read_b64_tr_b16 v[150:151], v202 offset:41472\n"
    "v_cvt_pk_bf16_f32 v81, v34, v35\n"
    "s_waitcnt lgkmcnt(4)\n"
    "v_mfma_f32_16x16x32_bf16 v[48:51], v[168:171], v[136:139], v[48:51]\n"
    "ds_read_b64_tr_b16 v[152:153], v201 offset:45056\n"
    "v_cvt_pk_bf16_f32 v82, v36, v37\n"
    "v_mfma_f32_16x16x32_bf16 v[52:55], v[172:175], v[136:139], v[52:55]\n"
    "ds_read_b64_tr_b16 v[154:155], v201 offset:45568\n"
    "v_cvt_pk_bf16_f32 v83, v38, v39\n"
    "v_mfma_f32_16x16x32_bf16 v[60:63], v[172:175], v[140:143], v[60:63]\n"
    "ds_read_b64_tr_b16 v[156:157], v202 offset:45056\n"
    "v_cvt_pk_bf16_f32 v86, v44, v45\n"
    "v_mfma_f32_16x16x32_bf16 v[56:59], v[168:171], v[140:143], v[56:59]\n"
    "ds_read_b64_tr_b16 v[158:159], v202 offset:45568\n"
    "v_cvt_pk_bf16_f32 v87, v46, v47\n"
    "s_waitcnt lgkmcnt(4)\n"
    "v_mfma_f32_16x16x32_bf16 v[48:51], v[176:179], v[144:147], v[48:51]\n"
    "ds_read_b64_tr_b16 v[128:129], v203 offset:16384\n"
    "v_cvt_pk_bf16_f32 v84, v40, v41\n"
    "v_mfma_f32_16x16x32_bf16 v[52:55], v[180:183], v[144:147], v[52:55]\n"
    "ds_read_b64_tr_b16 v[130:131], v203 offset:16896\n"
    "v_cvt_pk_bf16_f32 v85, v42, v43\n"
    "v_mfma_f32_16x16x32_bf16 v[60:63], v[180:183], v[148:151], v[60:63]\n"
    "ds_read_b64_tr_b16 v[132:133], v204 offset:16384\n"
    "v_mfma_f32_16x16x32_bf16 v[56:59], v[176:179], v[148:151], v[56:59]\n"
    "ds_read_b64_tr_b16 v[134:135], v204 offset:16896\n"
    "s_waitcnt lgkmcnt(4)\n"
    "v_mfma_f32_16x16x32_bf16 v[48:51], v[184:187], v[152:155], v[48:51]\n"
    "ds_read_b64_tr_b16 v[136:137], v203 offset:20480\n"
    "v_mfma_f32_16x16x32_bf16 v[52:55], v[188:191], v[152:155], v[52:55]\n"
    "ds_read_b64_tr_b16 v[138:139], v203 offset:20992\n"
    "v_mfma_f32_16x16x32_bf16 v[60:63], v[188:191], v[156:159], v[60:63]\n"
    "ds_read_b64_tr_b16 v[140:141], v204 offset:20480\n"
    "v_mfma_f32_16x16x32_bf16 v[56:59], v[184:187], v[156:159], v[56:59]\n"
    "ds_read_b64_tr_b16 v[142:143], v204 offset:20992\n"
    "s_waitcnt lgkmcnt(4)\n"
    "v_mfma_f32_16x16x32_bf16 v[32:35], v[160:163], v[128:131], 0\n"
    "ds_read_b64_tr_b16 v[144:145], v203 offset:24576\n"
    "v_mfma_f32_16x16x32_bf16 v[36:39], v[164:167], v[128:131], 0\n"
    "ds_read_b64_tr_b16 v[146:147], v203 offset:25088\n"
    "v_mfma_f32_16x16x32_bf16 v[44:47], v[164:167], v[132:135], 0\n"
    "ds_read_b64_tr_b16 v[148:149], v204 offset:24576\n"
    "v_cvt_pk_bf16_f32 v88, v48, v49\n"
    "v_mfma_f32_16x16x32_bf16 v[40:43], v[160:163], v[132:135], 0\n"
    "ds_read_b64_tr_b16 v[150:151], v204 offset:25088\n"
    "v_cvt_pk_bf16_f32 v89, v50, v51\n"
    "s_waitcnt lgkmcnt(4)\n"
    "v_mfma_f32_16x16x32_bf16 v[32:35], v[168:171], v[136:139], v[32:35]\n"
    "ds_read_b64_tr_b16 v[152:153], v203 offset:28672\n"
    "v_cvt_pk_bf16_f32 v90, v52, v53\n"
    "v_mfma_f32_16x16x32_bf16 v[36:39], v[172:175], v[136:139], v[36:39]\n"
    "ds_read_b64_tr_b16 v[154:155], v203 offset:29184\n"
    "v_cvt_pk_bf16_f32 v91, v54, v55\n"
    "v_mfma_f32_16x16x32_bf16 v[44:47], v[172:175], v[140:143], v[44:47]\n"
    "ds_read_b64_tr_b16 v[156:157], v204 offset:28672\n"
    "v_cvt_pk_bf16_f32 v94, v60, v61\n"
    "v_mfma_f32_16x16x32_bf16 v[40:43], v[168:171], v[140:143], v[40:43]\n"
    "ds_read_b64_tr_b16 v[158:159], v204 offset:29184\n"
    "v_cvt_pk_bf16_f32 v95, v62, v63\n"
    "s_waitcnt lgkmcnt(4)\n"
    "v_mfma_f32_16x16x32_bf16 v[32:35], v[176:179], v[144:147], v[32:35]\n"
    "ds_read_b64_tr_b16 v[128:129], v205 offset:16384\n"
    "v_cvt_pk_bf16_f32 v92, v56, v57\n"
    "v_mfma_f32_16x16x32_bf16 v[36:39], v[180:183], v[144:147], v[36:39]\n"
    "ds_read_b64_tr_b16 v[130:131], v205 offset:16896\n"
    "v_cvt_pk_bf16_f32 v93, v58, v59\n"
    "v_mfma_f32_16x16x32_bf16 v[44:47], v[180:183], v[148:151], v[44:47]\n"
    "ds_read_b64_tr_b16 v[132:133], v206 offset:16384\n"
    "v_mfma_f32_16x16x32_bf16 v[40:43], v[176:179], v[148:151], v[40:43]\n"
    "ds_read_b64_tr_b16 v[134:135], v206 offset:16896\n"
    "s_waitcnt lgkmcnt(4)\n"
    "v_mfma_f32_16x16x32_bf16 v[32:35], v[184:187], v[152:155], v[32:35]\n"
    "ds_read_b64_tr_b16 v[136:137], v205 offset:20480\n"
    "v_mfma_f32_16x16x32_bf16 v[36:39], v[188:191], v[152:155], v[36:39]\n"
    "ds_read_b64_tr_b16 v[138:139], v205 offset:20992\n"
    "v_mfma_f32_16x16x32_bf16 v[44:47], v[188:191], v[156:159], v[44:47]\n"
    "ds_read_b64_tr_b16 v[140:141], v206 offset:20480\n"
    "v_mfma_f32_16x16x32_bf16 v[40:43], v[184:187], v[156:159], v[40:43]\n"
    "ds_read_b64_tr_b16 v[142:143], v206 offset:20992\n"
    "s_waitcnt lgkmcnt(4)\n"
    "v_mfma_f32_16x16x32_bf16 v[48:51], v[160:163], v[128:131], 0\n"
    "ds_read_b64_tr_b16 v[144:145], v205 offset:24576\n"
    "v_mfma_f32_16x16x32_bf16 v[52:55], v[164:167], v[128:131], 0\n"
    "ds_read_b64_tr_b16 v[146:147], v205 offset:25088\n"
    "v_mfma_f32_16x16x32_bf16 v[60:63], v[164:167], v[132:135], 0\n"
    "ds_read_b64_tr_b16 v[148:149], v206 offset:24576\n"
    "v_cvt_pk_bf16_f32 v96, v32, v33\n"
    "v_mfma_f32_16x16x32_bf16 v[56:59], v[160:163], v[132:135], 0\n"
    "ds_read_b64_tr_b16 v[150:151], v206 offset:25088\n"
    "v_cvt_pk_bf16_f32 v97, v34, v35\n"
    "s_waitcnt lgkmcnt(4)\n"
    "v_mfma_f32_16x16x32_bf16 v[48:51], v[168:171], v[136:139], v[48:51]\n"
    "ds_read_b64_tr_b16 v[152:153], v205 offset:28672\n"
    "v_cvt_pk_bf16_f32 v98, v36, v37\n"
    "v_mfma_f32_16x16x32_bf16 v[52:55], v[172:175], v[136:139], v[52:55]\n"
    "ds_read_b64_tr_b16 v[154:155], v205 offset:29184\n"
    "v_cvt_pk_bf16_f32 v99, v38, v39\n"
    "v_mfma_f32_16x16x32_bf16 v[60:63], v[172:175], v[140:143], v[60:63]\n"
    "ds_read_b64_tr_b16 v[156:157], v206 offset:28672\n"
    "v_cvt_pk_bf16_f32 v102, v44, v45\n"
    "v_mfma_f32_16x16x32_bf16 v[56:59], v[168:171], v[140:143], v[56:59]\n"
    "ds_read_b64_tr_b16 v[158:159], v206 offset:29184\n"
    "v_cvt_pk_bf16_f32 v103, v46, v47\n"
    "s_waitcnt lgkmcnt(4)\n"
    "v_mfma_f32_16x16x32_bf16 v[48:51], v[176:179], v[144:147], v[48:51]\n"
    "s_barrier\n"
    "ds_read_b64_tr_b16 v[128:129], v203 offset:32768\n"
    "v_cvt_pk_bf16_f32 v100, v40, v41\n"
    "v_mfma_f32_16x16x32_bf16 v[52:55], v[180:183], v[144:147], v[52:55]\n"
    "ds_read_b64_tr_b16 v[130:131], v203 offset:33280\n"
    "v_cvt_pk_bf16_f32 v101, v42, v43\n"
    "v_mfma_f32_16x16x32_bf16 v[60:63], v[180:183], v[148:151], v[60:63]\n"
    "ds_read_b64_tr_b16 v[132:133], v204 offset:32768\n"
    "v_mfma_f32_16x16x32_bf16 v[56:59], v[176:179], v[148:151], v[56:59]\n"
    "ds_read_b64_tr_b16 v[134:135], v204 offset:33280\n"
    "s_waitcnt lgkmcnt(4)\n"
    "v_mfma_f32_16x16x32_bf16 v[48:51], v[184:187], v[152:155], v[48:51]\n"
    "ds_read_b64_tr_b16 v[136:137], v203 offset:36864\n"
    "v_mfma_f32_16x16x32_bf16 v[52:55], v[188:191], v[152:155], v[52:55]\n"
    "ds_read_b64_tr_b16 v[138:139], v203 offset:37376\n"
    "v_mfma_f32_16x16x32_bf16 v[60:63], v[188:191], v[156:159], v[60:63]\n"
    "ds_read_b64_tr_b16 v[140:141], v204 offset:36864\n"
    "v_mfma_f32_16x16x32_bf16 v[56:59], v[184:187], v[156:159], v[56:59]\n"
    "ds_read_b64_tr_b16 v[142:143], v204 offset:37376\n"
    "s_waitcnt lgkmcnt(4)\n"
    "v_mfma_f32_16x16x32_bf16 v[32:35], v[160:163], v[128:131], 0\n"
    "ds_read_b64_tr_b16 v[144:145], v203 offset:40960\n"
    "v_mfma_f32_16x16x32_bf16 v[36:39], v[164:167], v[128:131], 0\n"
    "ds_read_b64_tr_b16 v[146:147], v203 offset:41472\n"
    "v_mfma_f32_16x16x32_bf16 v[44:47], v[164:167], v[132:135], 0\n"
    "ds_read_b64_tr_b16 v[148:149], v204 offset:40960\n"
    "v_cvt_pk_bf16_f32 v104, v48, v49\n"
    "v_mfma_f32_16x16x32_bf16 v[40:43], v[160:163], v[132:135], 0\n"
    "ds_read_b64_tr_b16 v[150:151], v204 offset:41472\n"
    "v_cvt_pk_bf16_f32 v105, v50, v51\n"
    "s_waitcnt lgkmcnt(4)\n"
    "v_mfma_f32_16x16x32_bf16 v[32:35], v[168:171], v[136:139], v[32:35]\n"
    "ds_read_b64_tr_b16 v[152:153], v203 offset:45056\n"
    "v_cvt_pk_bf16_f32 v106, v52, v53\n"
    "v_mfma_f32_16x16x32_bf16 v[36:39], v[172:175], v[136:139], v[36:39]\n"
    "ds_read_b64_tr_b16 v[154:155], v203 offset:45568\n"
    "v_cvt_pk_bf16_f32 v107, v54, v55\n"
    "v_mfma_f32_16x16x32_bf16 v[44:47], v[172:175], v[140:143], v[44:47]\n"
    "ds_read_b64_tr_b16 v[156:157], v204 offset:45056\n"
    "v_cvt_pk_bf16_f32 v110, v60, v61\n"
    "v_mfma_f32_16x16x32_bf16 v[40:43], v[168:171], v[140:143], v[40:43]\n"
    "ds_read_b64_tr_b16 v[158:159], v204 offset:45568\n"
    "v_cvt_pk_bf16_f32 v111, v62, v63\n"
    "s_waitcnt lgkmcnt(4)\n"
    "v_mfma_f32_16x16x32_bf16 v[32:35], v[176:179], v[144:147], v[32:35]\n"
    "ds_read_b64_tr_b16 v[128:129], v205 offset:32768\n"
    "v_cvt_pk_bf16_f32 v108, v56, v57\n"
    "v_mfma_f32_16x16x32_bf16 v[36:39], v[180:183], v[144:147], v[36:39]\n"
    "ds_read_b64_tr_b16 v[130:131], v205 offset:33280\n"
    "v_cvt_pk_bf16_f32 v109, v58, v59\n"
    "v_mfma_f32_16x16x32_bf16 v[44:47], v[180:183], v[148:151], v[44:47]\n"
    "ds_read_b64_tr_b16 v[132:133], v206 offset:32768\n"
    "v_mfma_f32_16x16x32_bf16 v[40:43], v[176:179], v[148:151], v[40:43]\n"
    "ds_read_b64_tr_b16 v[134:135], v206 offset:33280\n"
    "s_waitcnt lgkmcnt(4)\n"
    "v_mfma_f32_16x16x32_bf16 v[32:35], v[184:187], v[152:155], v[32:35]\n"
    "ds_read_b64_tr_b16 v[136:137], v205 offset:36864\n"
    "v_mfma_f32_16x16x32_bf16 v[36:39], v[188:191], v[152:155], v[36:39]\n"
    "ds_read_b64_tr_b16 v[138:139], v205 offset:37376\n"
    "v_mfma_f32_16x16x32_bf16 v[44:47], v[188:191], v[156:159], v[44:47]\n"
    "ds_read_b64_tr_b16 v[140:141], v206 offset:36864\n"
    "v_mfma_f32_16x16x32_bf16 v[40:43], v[184:187], v[156:159], v[40:43]\n"
    "ds_read_b64_tr_b16 v[142:143], v206 offset:37376\n"
    "s_waitcnt lgkmcnt(4)\n"
    "v_mfma_f32_16x16x32_bf16 v[48:51], v[160:163], v[128:131], 0\n"
    "ds_read_b64_tr_b16 v[144:145], v205 offset:40960\n"
    "v_mfma_f32_16x16x32_bf16 v[52:55], v[164:167], v[128:131], 0\n"
    "ds_read_b64_tr_b16 v[146:147], v205 offset:41472\n"
    "v_mfma_f32_16x16x32_bf16 v[60:63], v[164:167], v[132:135], 0\n"
    "ds_read_b64_tr_b16 v[148:149], v206 offset:40960\n"
    "v_cvt_pk_bf16_f32 v112, v32, v33\n"
    "v_mfma_f32_16x16x32_bf16 v[56:59], v[160:163], v[132:135], 0\n"
    "ds_read_b64_tr_b16 v[150:151], v206 offset:41472\n"
    "v_cvt_pk_bf16_f32 v113, v34, v35\n"
    "s_waitcnt lgkmcnt(4)\n"
    "v_mfma_f32_16x16x32_bf16 v[48:51], v[168:171], v[136:139], v[48:51]\n"
    "ds_read_b64_tr_b16 v[152:153], v205 offset:45056\n"
    "v_cvt_pk_bf16_f32 v114, v36, v37\n"
    "v_mfma_f32_16x16x32_bf16 v[52:55], v[172:175], v[136:139], v[52:55]\n"
    "ds_read_b64_tr_b16 v[154:155], v205 offset:45568\n"
    "v_cvt_pk_bf16_f32 v115, v38, v39\n"
    "v_mfma_f32_16x16x32_bf16 v[60:63], v[172:175], v[140:143], v[60:63]\n"
    "ds_read_b64_tr_b16 v[156:157], v206 offset:45056\n"
    "v_cvt_pk_bf16_f32 v118, v44, v45\n"
    "v_mfma_f32_16x16x32_bf16 v[56:59], v[168:171], v[140:143], v[56:59]\n"
    "ds_read_b64_tr_b16 v[158:159], v206 offset:45568\n"
    "v_cvt_pk_bf16_f32 v119, v46, v47\n"
    "s_waitcnt lgkmcnt(4)\n"
    "v_mfma_f32_16x16x32_bf16 v[48:51], v[176:179], v[144:147], v[48:51]\n"
    "s_barrier\n"
    "ds_read_b64_tr_b16 v[128:129], v203 offset:0\n"
    "v_cvt_pk_bf16_f32 v116, v40, v41\n"
    "v_mfma_f32_16x16x32_bf16 v[52:55], v[180:183], v[144:147], v[52:55]\n"
    "ds_read_b64_tr_b16 v[130:131], v203 offset:512\n"
    "v_cvt_pk_bf16_f32 v117, v42, v43\n"
    "v_mfma_f32_16x16x32_bf16 v[60:63], v[180:183], v[148:151], v[60:63]\n"
    "ds_read_b64_tr_b16 v[132:133], v204 offset:0\n"
    "v_mfma_f32_16x16x32_bf16 v[56:59], v[176:179], v[148:151], v[56:59]\n"
    "ds_read_b64_tr_b16 v[134:135], v204 offset:512\n"
    "s_waitcnt lgkmcnt(4)\n"
    "v_mfma_f32_16x16x32_bf16 v[48:51], v[184:187], v[152:155], v[48:51]\n"
    "ds_read_b64_tr_b16 v[136:137], v203 offset:4096\n"
    "v_mfma_f32_16x16x32_bf16 v[52:55], v[188:191], v[152:155], v[52:55]\n"
    "ds_read_b64_tr_b16 v[138:139], v203 offset:4608\n"
    "v_mfma_f32_16x16x32_bf16 v[60:63], v[188:191], v[156:159], v[60:63]\n"
    "ds_read_b64_tr_b16 v[140:141], v204 offset:4096\n"
    "v_mfma_f32_16x16x32_bf16 v[56:59], v[184:187], v[156:159], v[56:59]\n"
    "ds_read_b64_tr_b16 v[142:143], v204 offset:4608\n"
    "s_waitcnt lgkmcnt(4)\n"
    "v_mfma_f32_16x16x32_bf16 v[32:35], v[160:163], v[128:131], 0\n"
    "ds_read_b64_tr_b16 v[144:145], v203 offset:8192\n"
    "v_mfma_f32_16x16x32_bf16 v[36:39], v[164:167], v[128:131], 0\n"
    "ds_read_b64_tr_b16 v[146:147], v203 offset:8704\n"
    "v_mfma_f32_16x16x32_bf16 v[44:47], v[164:167], v[132:135], 0\n"
    "ds_read_b64_tr_b16 v[148:149], v204 offset:8192\n"
    "v_cvt_pk_bf16_f32 v120, v48, v49\n"
    "v_mfma_f32_16x16x32_bf16 v[40:43], v[160:163], v[132:135], 0\n"
    "ds_read_b64_tr_b16 v[150:151], v204 offset:8704\n"
    "v_cvt_pk_bf16_f32 v121, v50, v51\n"
    "s_waitcnt lgkmcnt(4)\n"
    "v_mfma_f32_16x16x32_bf16 v[32:35], v[168:171], v[136:139], v[32:35]\n"
    "ds_read_b64_tr_b16 v[152:153], v203 offset:12288\n"
    "v_cvt_pk_bf16_f32 v122, v52, v53\n"
    "v_mfma_f32_16x16x32_bf16 v[36:39], v[172:175], v[136:139], v[36:39]\n"
    "ds_read_b64_tr_b16 v[154:155], v203 offset:12800\n"
    "v_cvt_pk_bf16_f32 v123, v54, v55\n"
    "v_mfma_f32_16x16x32_bf16 v[44:47], v[172:175], v[140:143], v[44:47]\n"
    "ds_read_b64_tr_b16 v[156:157], v204 offset:12288\n"
    "v_cvt_pk_bf16_f32 v126, v60, v61\n"
    "v_mfma_f32_16x16x32_bf16 v[40:43], v[168:171], v[140:143], v[40:43]\n"
    "ds_read_b64_tr_b16 v[158:159], v204 offset:12800\n"
    "v_cvt_pk_bf16_f32 v127, v62, v63\n"
    "s_waitcnt lgkmcnt(4)\n"
    "v_mfma_f32_16x16x32_bf16 v[32:35], v[176:179], v[144:147], v[32:35]\n"
    "ds_read_b64_tr_b16 v[128:129], v205 offset:0\n"
    "v_cvt_pk_bf16_f32 v124, v56, v57\n"
    "v_mfma_f32_16x16x32_bf16 v[36:39], v[180:183], v[144:147], v[36:39]\n"
    "ds_read_b64_tr_b16 v[130:131], v205 offset:512\n"
    "v_cvt_pk_bf16_f32 v125, v58, v59\n"
    "v_mfma_f32_16x16x32_bf16 v[44:47], v[180:183], v[148:151], v[44:47]\n"
    "ds_read_b64_tr_b16 v[132:133], v206 offset:0\n"
    "v_mfma_f32_16x16x32_bf16 v[40:43], v[176:179], v[148:151], v[40:43]\n"
    "ds_read_b64_tr_b16 v[134:135], v206 offset:512\n"
    "s_waitcnt lgkmcnt(4)\n"
    "v_mfma_f32_16x16x32_bf16 v[32:35], v[184:187], v[152:155], v[32:35]\n"
    "ds_read_b64_tr_b16 v[136:137], v205 offset:4096\n"
    "v_mfma_f32_16x16x32_bf16 v[36:39], v[188:191], v[152:155], v[36:39]\n"
    "ds_read_b64_tr_b16 v[138:139], v205 offset:4608\n"
    "v_mfma_f32_16x16x32_bf16 v[44:47], v[188:191], v[156:159], v[44:47]\n"
    "ds_read_b64_tr_b16 v[140:141], v206 offset:4096\n"
    "v_mfma_f32_16x16x32_bf16 v[40:43], v[184:187], v[156:159], v[40:43]\n"
    "ds_read_b64_tr_b16 v[142:143], v206 offset:4608\n"
    "s_waitcnt lgkmcnt(4)\n"
    "v_mfma_f32_16x16x32_bf16 v[48:51], v[160:163], v[128:131], 0\n"
    "ds_read_b64_tr_b16 v[144:145], v205 offset:8192\n"
    "v_mfma_f32_16x16x32_bf16 v[52:55], v[164:167], v[128:131], 0\n"
    "ds_read_b64_tr_b16 v[146:147], v205 offset:8704\n"
    "v_mfma_f32_16x16x32_bf16 v[60:63], v[164:167], v[132:135], 0\n"
    "ds_read_b64_tr_b16 v[148:149], v206 offset:8192\n"
    "v_cvt_pk_bf16_f32 v192, v32, v33\n"
    "v_mfma_f32_16x16x32_bf16 v[56:59], v[160:163], v[132:135], 0\n"
    "ds_read_b64_tr_b16 v[150:151], v206 offset:8704\n"
    "v_cvt_pk_bf16_f32 v193, v34, v35\n"
    "s_waitcnt lgkmcnt(4)\n"
    "v_mfma_f32_16x16x32_bf16 v[48:51], v[168:171], v[136:139], v[48:51]\n"
    "ds_read_b64_tr_b16 v[152:153], v205 offset:12288\n"
    "v_cvt_pk_bf16_f32 v194, v36, v37\n"
    "v_mfma_f32_16x16x32_bf16 v[52:55], v[172:175], v[136:139], v[52:55]\n"
    "ds_read_b64_tr_b16 v[154:155], v205 offset:12800\n"
    "v_cvt_pk_bf16_f32 v195, v38, v39\n"
    "v_mfma_f32_16x16x32_bf16 v[60:63], v[172:175], v[140:143], v[60:63]\n"
    "ds_read_b64_tr_b16 v[156:157], v206 offset:12288\n"
    "v_cvt_pk_bf16_f32 v198, v44, v45\n"
    "v_mfma_f32_16x16x32_bf16 v[56:59], v[168:171], v[140:143], v[56:59]\n"
    "ds_read_b64_tr_b16 v[158:159], v206 offset:12800\n"
    "v_cvt_pk_bf16_f32 v199, v46, v47\n"
    "s_waitcnt lgkmcnt(4)\n"
    "v_mfma_f32_16x16x32_bf16 v[48:51], v[176:179], v[144:147], v[48:51]\n"
    "ds_read_b64_tr_b16 v[128:129], v203 offset:49152\n"
    "v_cvt_pk_bf16_f32 v196, v40, v41\n"
    "v_mfma_f32_16x16x32_bf16 v[52:55], v[180:183], v[144:147], v[52:55]\n"
    "ds_read_b64_tr_b16 v[130:131], v203 offset:49664\n"
    "v_cvt_pk_bf16_f32 v197, v42, v43\n"
    "v_mfma_f32_16x16x32_bf16 v[60:63], v[180:183], v[148:151], v[60:63]\n"
    "ds_read_b64_tr_b16 v[132:133], v204 offset:49152\n"
    "v_mfma_f32_16x16x32_bf16 v[56:59], v[176:179], v[148:151], v[56:59]\n"
    "ds_read_b64_tr_b16 v[134:135], v204 offset:49664\n"
    "s_waitcnt lgkmcnt(4)\n"
    "v_mfma_f32_16x16x32_bf16 v[48:51], v[184:187], v[152:155], v[48:51]\n"
    "ds_read_b64_tr_b16 v[136:137], v203 offset:53248\n"
    "v_mfma_f32_16x16x32_bf16 v[52:55], v[188:191], v[152:155], v[52:55]\n"
    "ds_read_b64_tr_b16 v[138:139], v203 offset:53760\n"
    "v_mfma_f32_16x16x32_bf16 v[60:63], v[188:191], v[156:159], v[60:63]\n"
    "ds_read_b64_tr_b16 v[140:141], v204 offset:53248\n"
    "v_mfma_f32_16x16x32_bf16 v[56:59], v[184:187], v[156:159], v[56:59]\n"
    "ds_read_b64_tr_b16 v[142:143], v204 offset:53760\n"
    "s_waitcnt lgkmcnt(4)\n"
    "v_mfma_f32_16x16x32_bf16 v[32:35], v[160:163], v[128:131], 0\n"
    "ds_read_b64_tr_b16 v[144:145], v203 offset:57344\n"
    "v_mfma_f32_16x16x32_bf16 v[36:39], v[164:167], v[128:131], 0\n"
    "ds_read_b64_tr_b16 v[146:147], v203 offset:57856\n"
    "v_mfma_f32_16x16x32_bf16 v[44:47], v[164:167], v[132:135], 0\n"
    "ds_read_b64_tr_b16 v[148:149], v204 offset:57344\n"
    "v_cvt_pk_bf16_f32 v220, v48, v49\n"
    "v_mfma_f32_16x16x32_bf16 v[40:43], v[160:163], v[132:135], 0\n"
    "ds_read_b64_tr_b16 v[150:151], v204 offset:57856\n"
    "v_cvt_pk_bf16_f32 v221, v50, v51\n"
    "s_waitcnt lgkmcnt(4)\n"
    "v_mfma_f32_16x16x32_bf16 v[32:35], v[168:171], v[136:139], v[32:35]\n"
    "ds_read_b64_tr_b16 v[152:153], v203 offset:61440\n"
    "v_cvt_pk_bf16_f32 v222, v52, v53\n"
    "v_mfma_f32_16x16x32_bf16 v[36:39], v[172:175], v[136:139], v[36:39]\n"
    "ds_read_b64_tr_b16 v[154:155], v203 offset:61952\n"
    "v_cvt_pk_bf16_f32 v223, v54, v55\n"
    "v_mfma_f32_16x16x32_bf16 v[44:47], v[172:175], v[140:143], v[44:47]\n"
    "ds_read_b64_tr_b16 v[156:157], v204 offset:61440\n"
    "v_cvt_pk_bf16_f32 v226, v60, v61\n"
    "v_mfma_f32_16x16x32_bf16 v[40:43], v[168:171], v[140:143], v[40:43]\n"
    "ds_read_b64_tr_b16 v[158:159], v204 offset:61952\n"
    "v_cvt_pk_bf16_f32 v227, v62, v63\n"
    "s_waitcnt lgkmcnt(4)\n"
    "v_mfma_f32_16x16x32_bf16 v[32:35], v[176:179], v[144:147], v[32:35]\n"
    "ds_read_b64_tr_b16 v[128:129], v205 offset:49152\n"
    "v_cvt_pk_bf16_f32 v224, v56, v57\n"
    "v_mfma_f32_16x16x32_bf16 v[36:39], v[180:183], v[144:147], v[36:39]\n"
    "ds_read_b64_tr_b16 v[130:131], v205 offset:49664\n"
    "v_cvt_pk_bf16_f32 v225, v58, v59\n"
    "v_mfma_f32_16x16x32_bf16 v[44:47], v[180:183], v[148:151], v[44:47]\n"
    "ds_read_b64_tr_b16 v[132:133], v206 offset:49152\n"
    "v_mfma_f32_16x16x32_bf16 v[40:43], v[176:179], v[148:151], v[40:43]\n"
    "ds_read_b64_tr_b16 v[134:135], v206 offset:49664\n"
    "s_waitcnt lgkmcnt(4)\n"
    "v_mfma_f32_16x16x32_bf16 v[32:35], v[184:187], v[152:155], v[32:35]\n"
    "ds_read_b64_tr_b16 v[136:137], v205 offset:53248\n"
    "v_mfma_f32_16x16x32_bf16 v[36:39], v[188:191], v[152:155], v[36:39]\n"
    "ds_read_b64_tr_b16 v[138:139], v205 offset:53760\n"
    "v_mfma_f32_16x16x32_bf16 v[44:47], v[188:191], v[156:159], v[44:47]\n"
    "ds_read_b64_tr_b16 v[140:141], v206 offset:53248\n"
    "v_mfma_f32_16x16x32_bf16 v[40:43], v[184:187], v[156:159], v[40:43]\n"
    "ds_read_b64_tr_b16 v[142:143], v206 offset:53760\n"
    "s_waitcnt lgkmcnt(4)\n"
    "v_mfma_f32_16x16x32_bf16 v[48:51], v[160:163], v[128:131], 0\n"
    "ds_read_b64_tr_b16 v[144:145], v205 offset:57344\n"
    "v_mfma_f32_16x16x32_bf16 v[52:55], v[164:167], v[128:131], 0\n"
    "ds_read_b64_tr_b16 v[146:147], v205 offset:57856\n"
    "v_mfma_f32_16x16x32_bf16 v[60:63], v[164:167], v[132:135], 0\n"
    "ds_read_b64_tr_b16 v[148:149], v206 offset:57344\n"
    "v_cvt_pk_bf16_f32 v228, v32, v33\n"
    "v_mfma_f32_16x16x32_bf16 v[56:59], v[160:163], v[132:135], 0\n"
    "ds_read_b64_tr_b16 v[150:151], v206 offset:57856\n"
    "v_cvt_pk_bf16_f32 v229, v34, v35\n"
    "s_waitcnt lgkmcnt(4)\n"
    "v_mfma_f32_16x16x32_bf16 v[48:51], v[168:171], v[136:139], v[48:51]\n"
    "ds_read_b64_tr_b16 v[152:153], v205 offset:61440\n"
    "v_cvt_pk_bf16_f32 v230, v36, v37\n"
    "v_mfma_f32_16x16x32_bf16 v[52:55], v[172:175], v[136:139], v[52:55]\n"
    "ds_read_b64_tr_b16 v[154:155], v205 offset:61952\n"
    "v_cvt_pk_bf16_f32 v231, v38, v39\n"
    "v_mfma_f32_16x16x32_bf16 v[60:63], v[172:175], v[140:143], v[60:63]\n"
    "ds_read_b64_tr_b16 v[156:157], v206 offset:61440\n"
    "v_cvt_pk_bf16_f32 v234, v44, v45\n"
    "v_mfma_f32_16x16x32_bf16 v[56:59], v[168:171], v[140:143], v[56:59]\n"
    "ds_read_b64_tr_b16 v[158:159], v206 offset:61952\n"
    "v_cvt_pk_bf16_f32 v235, v46, v47\n"
    "s_waitcnt lgkmcnt(4)\n"
    "v_mfma_f32_16x16x32_bf16 v[48:51], v[176:179], v[144:147], v[48:51]\n"
    "s_barrier\n"
    "ds_read_b64_tr_b16 v[128:129], v210 offset:0\n"
    "v_cvt_pk_bf16_f32 v232, v40, v41\n"
    "v_mfma_f32_16x16x32_bf16 v[52:55], v[180:183], v[144:147], v[52:55]\n"
    "ds_read_b64_tr_b16 v[130:131], v210 offset:512\n"
    "v_cvt_pk_bf16_f32 v233, v42, v43\n"
    "v_mfma_f32_16x16x32_bf16 v[60:63], v[180:183], v[148:151], v[60:63]\n"
    "ds_read_b64_tr_b16 v[132:133], v200 offset:0\n"
    "v_mfma_f32_16x16x32_bf16 v[56:59], v[176:179], v[148:151], v[56:59]\n"
    "ds_read_b64_tr_b16 v[134:135], v200 offset:512\n"
    "s_waitcnt lgkmcnt(4)\n"
    "v_mfma_f32_16x16x32_bf16 v[48:51], v[184:187], v[152:155], v[48:51]\n"
    "ds_read_b64_tr_b16 v[136:137], v210 offset:4096\n"
    "ds_read_b128 v[236:239], v212 offset:0\n"
    "v_mfma_f32_16x16x32_bf16 v[52:55], v[188:191], v[152:155], v[52:55]\n"
    "ds_read_b64_tr_b16 v[138:139], v210 offset:4608\n"
    "ds_read_b128 v[240:243], v212 offset:1024\n"
    "v_mfma_f32_16x16x32_bf16 v[60:63], v[188:191], v[156:159], v[60:63]\n"
    "ds_read_b64_tr_b16 v[140:141], v200 offset:4096\n"
    "v_mfma_f32_16x16x32_bf16 v[56:59], v[184:187], v[156:159], v[56:59]\n"
    "ds_read_b64_tr_b16 v[142:143], v200 offset:4608\n"
    "s_waitcnt lgkmcnt(6)\n"
    "v_mfma_f32_16x16x32_bf16 v[32:35], v[160:163], v[128:131], 0\n"
    "ds_read_b64_tr_b16 v[144:145], v210 offset:8192\n"
    "v_mfma_f32_16x16x32_bf16 v[36:39], v[164:167], v[128:131], 0\n"
    "ds_read_b64_tr_b16 v[146:147], v210 offset:8704\n"
    "v_mfma_f32_16x16x32_bf16 v[44:47], v[164:167], v[132:135], 0\n"
    "ds_read_b64_tr_b16 v[148:149], v200 offset:8192\n"
    "v_cvt_pk_bf16_f32 v244, v48, v49\n"
    "v_mfma_f32_16x16x32_bf16 v[40:43], v[160:163], v[132:135], 0\n"
    "ds_read_b64_tr_b16 v[150:151], v200 offset:8704\n"
    "v_cvt_pk_bf16_f32 v245, v50, v51\n"
    "s_waitcnt lgkmcnt(6)\n"
    "v_mfma_f32_16x16x32_bf16 v[0:3], v[64:67], v[236:239], 0\n"
    "v_mfma_f32_16x16x32_bf16 v[8:11], v[64:67], v[240:243], 0\n"
    "v_cvt_pk_bf16_f32 v246, v52, v53\n"
    "s_waitcnt lgkmcnt(4)\n"
    "v_mfma_f32_16x16x32_bf16 v[32:35], v[168:171], v[136:139], v[32:35]\n"
    "ds_read_b64_tr_b16 v[152:153], v210 offset:12288\n"
    "v_cvt_pk_bf16_f32 v247, v54, v55\n"
    "v_mfma_f32_16x16x32_bf16 v[36:39], v[172:175], v[136:139], v[36:39]\n"
    "ds_read_b64_tr_b16 v[154:155], v210 offset:12800\n"
    "v_mfma_f32_16x16x32_bf16 v[44:47], v[172:175], v[140:143], v[44:47]\n"
    "ds_read_b64_tr_b16 v[156:157], v200 offset:12288\n"
    "v_cvt_pk_bf16_f32 v250, v60, v61\n"
    "v_mfma_f32_16x16x32_bf16 v[40:43], v[168:171], v[140:143], v[40:43]\n"
    "ds_read_b64_tr_b16 v[158:159], v200 offset:12800\n"
    "v_cvt_pk_bf16_f32 v251, v62, v63\n"
    "v_mfma_f32_16x16x32_bf16 v[12:15], v[68:71], v[240:243], 0\n"
    "v_mfma_f32_16x16x32_bf16 v[4:7], v[68:71], v[236:239], 0\n"
    "v_cvt_pk_bf16_f32 v248, v56, v57\n"
    "s_waitcnt lgkmcnt(4)\n"
    "v_mfma_f32_16x16x32_bf16 v[32:35], v[176:179], v[144:147], v[32:35]\n"
    "ds_read_b64_tr_b16 v[128:129], v201 offset:0\n"
    "v_cvt_pk_bf16_f32 v249, v58, v59\n"
    "v_mfma_f32_16x16x32_bf16 v[36:39], v[180:183], v[144:147], v[36:39]\n"
    "ds_read_b64_tr_b16 v[130:131], v201 offset:512\n"
    "v_mfma_f32_16x16x32_bf16 v[44:47], v[180:183], v[148:151], v[44:47]\n"
    "ds_read_b64_tr_b16 v[132:133], v202 offset:0\n"
    "v_mfma_f32_16x16x32_bf16 v[40:43], v[176:179], v[148:151], v[40:43]\n"
    "ds_read_b64_tr_b16 v[134:135], v202 offset:512\n"
    "v_mfma_f32_16x16x32_bf16 v[16:19], v[72:75], v[236:239], 0\n"
    "v_mfma_f32_16x16x32_bf16 v[24:27], v[72:75], v[240:243], 0\n"
    "s_waitcnt lgkmcnt(4)\n"
    "v_mfma_f32_16x16x32_bf16 v[32:35], v[184:187], v[152:155], v[32:35]\n"
    "ds_read_b64_tr_b16 v[136:137], v201 offset:4096\n"
    "ds_read_b128 v[64:67], v212 offset:8192\n"
    "v_mfma_f32_16x16x32_bf16 v[36:39], v[188:191], v[152:155], v[36:39]\n"
    "ds_read_b64_tr_b16 v[138:139], v201 offset:4608\n"
    "ds_read_b128 v[68:71], v212 offset:9216\n"
    "v_mfma_f32_16x16x32_bf16 v[44:47], v[188:191], v[156:159], v[44:47]\n"
    "ds_read_b64_tr_b16 v[140:141], v202 offset:4096\n"
    "v_mfma_f32_16x16x32_bf16 v[40:43], v[184:187], v[156:159], v[40:43]\n"
    "ds_read_b64_tr_b16 v[142:143], v202 offset:4608\n"
    "v_mfma_f32_16x16x32_bf16 v[28:31], v[76:79], v[240:243], 0\n"
    "v_mfma_f32_16x16x32_bf16 v[20:23], v[76:79], v[236:239], 0\n"
    "s_waitcnt lgkmcnt(6)\n"
    "v_mfma_f32_16x16x32_bf16 v[48:51], v[160:163], v[128:131], 0\n"
    "ds_read_b64_tr_b16 v[144:145], v201 offset:8192\n"
    "v_mfma_f32_16x16x32_bf16 v[52:55], v[164:167], v[128:131], 0\n"
    "ds_read_b64_tr_b16 v[146:147], v201 offset:8704\n"
    "v_mfma_f32_16x16x32_bf16 v[60:63], v[164:167], v[132:135], 0\n"
    "ds_read_b64_tr_b16 v[148:149], v202 offset:8192\n"
    "v_cvt_pk_bf16_f32 v72, v32, v33\n"
    "v_mfma_f32_16x16x32_bf16 v[56:59], v[160:163], v[132:135], 0\n"
    "ds_read_b64_tr_b16 v[150:151], v202 offset:8704\n"
    "v_cvt_pk_bf16_f32 v73, v34, v35\n"
    "s_waitcnt lgkmcnt(6)\n"
    "v_mfma_f32_16x16x32_bf16 v[0:3], v[80:83], v[64:67], v[0:3]\n"
    "v_mfma_f32_16x16x32_bf16 v[8:11], v[80:83], v[68:71], v[8:11]\n"
    "v_cvt_pk_bf16_f32 v74, v36, v37\n"
    "s_waitcnt lgkmcnt(4)\n"
    "v_mfma_f32_16x16x32_bf16 v[48:51], v[168:171], v[136:139], v[48:51]\n"
    "ds_read_b64_tr_b16 v[152:153], v201 offset:12288\n"
    "v_cvt_pk_bf16_f32 v75, v38, v39\n"
    "v_mfma_f32_16x16x32_bf16 v[52:55], v[172:175], v[136:139], v[52:55]\n"
    "ds_read_b64_tr_b16 v[154:155], v201 offset:12800\n"
    "v_mfma_f32_16x16x32_bf16 v[60:63], v[172:175], v[140:143], v[60:63]\n"
    "ds_read_b64_tr_b16 v[156:157], v202 offset:12288\n"
    "v_cvt_pk_bf16_f32 v78, v44, v45\n"
    "v_mfma_f32_16x16x32_bf16 v[56:59], v[168:171], v[140:143], v[56:59]\n"
    "ds_read_b64_tr_b16 v[158:159], v202 offset:12800\n"
    "v_cvt_pk_bf16_f32 v79, v46, v47\n"
    "v_mfma_f32_16x16x32_bf16 v[12:15], v[84:87], v[68:71], v[12:15]\n"
    "v_mfma_f32_16x16x32_bf16 v[4:7], v[84:87], v[64:67], v[4:7]\n"
    "v_cvt_pk_bf16_f32 v76, v40, v41\n"
    "s_waitcnt lgkmcnt(4)\n"
    "v_mfma_f32_16x16x32_bf16 v[48:51], v[176:179], v[144:147], v[48:51]\n"
    "s_barrier\n"
    "ds_read_b64_tr_b16 v[128:129], v210 offset:16384\n"
    "v_cvt_pk_bf16_f32 v77, v42, v43\n"
    "v_mfma_f32_16x16x32_bf16 v[52:55], v[180:183], v[144:147], v[52:55]\n"
    "ds_read_b64_tr_b16 v[130:131], v210 offset:16896\n"
    "v_mfma_f32_16x16x32_bf16 v[60:63], v[180:183], v[148:151], v[60:63]\n"
    "ds_read_b64_tr_b16 v[132:133], v200 offset:16384\n"
    "v_mfma_f32_16x16x32_bf16 v[56:59], v[176:179], v[148:151], v[56:59]\n"
    "ds_read_b64_tr_b16 v[134:135], v200 offset:16896\n"
    "v_mfma_f32_16x16x32_bf16 v[16:19], v[88:91], v[64:67], v[16:19]\n"
    "v_mfma_f32_16x16x32_bf16 v[24:27], v[88:91], v[68:71], v[24:27]\n"
    "s_waitcnt lgkmcnt(4)\n"
    "v_mfma_f32_16x16x32_bf16 v[48:51], v[184:187], v[152:155], v[48:51]\n"
    "ds_read_b64_tr_b16 v[136:137], v210 offset:20480\n"
    "ds_read_b128 v[80:83], v212 offset:16384\n"
    "v_mfma_f32_16x16x32_bf16 v[52:55], v[188:191], v[152:155], v[52:55]\n"
    "ds_read_b64_tr_b16 v[138:139], v210 offset:20992\n"
    "ds_read_b128 v[84:87], v212 offset:17408\n"
    "v_mfma_f32_16x16x32_bf16 v[60:63], v[188:191], v[156:159], v[60:63]\n"
    "ds_read_b64_tr_b16 v[140:141], v200 offset:20480\n"
    "v_mfma_f32_16x16x32_bf16 v[56:59], v[184:187], v[156:159], v[56:59]\n"
    "ds_read_b64_tr_b16 v[142:143], v200 offset:20992\n"
    "v_mfma_f32_16x16x32_bf16 v[28:31], v[92:95], v[68:71], v[28:31]\n"
    "v_mfma_f32_16x16x32_bf16 v[20:23], v[92:95], v[64:67], v[20:23]\n"
    "s_waitcnt lgkmcnt(6)\n"
    "v_mfma_f32_16x16x32_bf16 v[32:35], v[160:163], v[128:131], 0\n"
    "ds_read_b64_tr_b16 v[144:145], v210 offset:24576\n"
    "v_mfma_f32_16x16x32_bf16 v[36:39], v[164:167], v[128:131], 0\n"
    "ds_read_b64_tr_b16 v[146:147], v210 offset:25088\n"
    "v_mfma_f32_16x16x32_bf16 v[44:47], v[164:167], v[132:135], 0\n"
    "ds_read_b64_tr_b16 v[148:149], v200 offset:24576\n"
    "v_cvt_pk_bf16_f32 v64, v48, v49\n"
    "v_mfma_f32_16x16x32_bf16 v[40:43], v[160:163], v[132:135], 0\n"
    "ds_read_b64_tr_b16 v[150:151], v200 offset:25088\n"
    "v_cvt_pk_bf16_f32 v65, v50, v51\n"
    "s_waitcnt lgkmcnt(6)\n"
    "v_mfma_f32_16x16x32_bf16 v[0:3], v[96:99], v[80:83], v[0:3]\n"
    "v_mfma_f32_16x16x32_bf16 v[8:11], v[96:99], v[84:87], v[8:11]\n"
    "v_cvt_pk_bf16_f32 v66, v52, v53\n"
    "s_waitcnt lgkmcnt(4)\n"
    "v_mfma_f32_16x16x32_bf16 v[32:35], v[168:171], v[136:139], v[32:35]\n"
    "ds_read_b64_tr_b16 v[152:153], v210 offset:28672\n"
    "v_cvt_pk_bf16_f32 v67, v54, v55\n"
    "v_mfma_f32_16x16x32_bf16 v[36:39], v[172:175], v[136:139], v[36:39]\n"
    "ds_read_b64_tr_b16 v[154:155], v210 offset:29184\n"
    "v_mfma_f32_16x16x32_bf16 v[44:47], v[172:175], v[140:143], v[44:47]\n"
    "ds_read_b64_tr_b16 v[156:157], v200 offset:28672\n"
    "v_cvt_pk_bf16_f32 v70, v60, v61\n"
    "v_mfma_f32_16x16x32_bf16 v[40:43], v[168:171], v[140:143], v[40:43]\n"
    "ds_read_b64_tr_b16 v[158:159], v200 offset:29184\n"
    "v_cvt_pk_bf16_f32 v71, v62, v63\n"
    "v_mfma_f32_16x16x32_bf16 v[12:15], v[100:103], v[84:87], v[12:15]\n"
    "v_mfma_f32_16x16x32_bf16 v[4:7], v[100:103], v[80:83], v[4:7]\n"
    "v_cvt_pk_bf16_f32 v68, v56, v57\n"
    "s_waitcnt lgkmcnt(4)\n"
    "v_mfma_f32_16x16x32_bf16 v[32:35], v[176:179], v[144:147], v[32:35]\n"
    "ds_read_b64_tr_b16 v[128:129], v201 offset:16384\n"
    "v_cvt_pk_bf16_f32 v69, v58, v59\n"
    "v_mfma_f32_16x16x32_bf16 v[36:39], v[180:183], v[144:147], v[36:39]\n"
    "ds_read_b64_tr_b16 v[130:131], v201 offset:16896\n"
    "v_mfma_f32_16x16x32_bf16 v[44:47], v[180:183], v[148:151], v[44:47]\n"
    "ds_read_b64_tr_b16 v[132:133], v202 offset:16384\n"
    "v_mfma_f32_16x16x32_bf16 v[40:43], v[176:179], v[148:151], v[40:43]\n"
    "ds_read_b64_tr_b16 v[134:135], v202 offset:16896\n"
    "v_mfma_f32_16x16x32_bf16 v[16:19], v[104:107], v[80:83], v[16:19]\n"
    "v_mfma_f32_16x16x32_bf16 v[24:27], v[104:107], v[84:87], v[24:27]\n"
    "s_waitcnt lgkmcnt(4)\n"
    "v_mfma_f32_16x16x32_bf16 v[32:35], v[184:187], v[152:155], v[32:35]\n"
    "ds_read_b64_tr_b16 v[136:137], v201 offset:20480\n"
    "ds_read_b128 v[88:91], v212 offset:24576\n"
    "v_mfma_f32_16x16x32_bf16 v[36:39], v[188:191], v[152:155], v[36:39]\n"
    "ds_read_b64_tr_b16 v[138:139], v201 offset:20992\n"
    "ds_read_b128 v[92:95], v212 offset:25600\n"
    "v_mfma_f32_16x16x32_bf16 v[44:47], v[188:191], v[156:159], v[44:47]\n"
    "ds_read_b64_tr_b16 v[140:141], v202 offset:20480\n"
    "v_mfma_f32_16x16x32_bf16 v[40:43], v[184:187], v[156:159], v[40:43]\n"
    "ds_read_b64_tr_b16 v[142:143], v202 offset:20992\n"
    "v_mfma_f32_16x16x32_bf16 v[28:31], v[108:111], v[84:87], v[28:31]\n"
    "v_mfma_f32_16x16x32_bf16 v[20:23], v[108:111], v[80:83], v[20:23]\n"
    "s_waitcnt lgkmcnt(6)\n"
    "v_mfma_f32_16x16x32_bf16 v[48:51], v[160:163], v[128:131], 0\n"
    "ds_read_b64_tr_b16 v[144:145], v201 offset:24576\n"
    "s_cmp_eq_u32 s69, 0\n"
    "s_mov_b32 s67, 0x14000\n"
    "s_cselect_b32 s67, 0x10000, s67\n"
    "v_mfma_f32_16x16x32_bf16 v[52:55], v[164:167], v[128:131], 0\n"
    "ds_read_b64_tr_b16 v[146:147], v201 offset:25088\n"
    "v_xor_b32 v252, 0x0, v211\n"
    "v_add_u32 v252, s67, v252\n"
    "v_mfma_f32_16x16x32_bf16 v[60:63], v[164:167], v[132:135], 0\n"
    "ds_read_b64_tr_b16 v[148:149], v202 offset:24576\n"
    "v_cvt_pk_bf16_f32 v80, v32, v33\n"
    "v_mfma_f32_16x16x32_bf16 v[56:59], v[160:163], v[132:135], 0\n"
    "ds_read_b64_tr_b16 v[150:151], v202 offset:25088\n"
    "v_cvt_pk_bf16_f32 v81, v34, v35\n"
    "s_waitcnt lgkmcnt(6)\n"
    "v_mfma_f32_16x16x32_bf16 v[0:3], v[112:115], v[88:91], v[0:3]\n"
    "ds_read_b128 v[160:163], v252 offset:0\n"
    "ds_read_b128 v[164:167], v252 offset:4096\n"
    "v_mfma_f32_16x16x32_bf16 v[8:11], v[112:115], v[92:95], v[8:11]\n"
    "v_cvt_pk_bf16_f32 v82, v36, v37\n"
    "s_waitcnt lgkmcnt(6)\n"
    "v_mfma_f32_16x16x32_bf16 v[48:51], v[168:171], v[136:139], v[48:51]\n"
    "ds_read_b64_tr_b16 v[152:153], v201 offset:28672\n"
    "v_cvt_pk_bf16_f32 v83, v38, v39\n"
    "v_mfma_f32_16x16x32_bf16 v[52:55], v[172:175], v[136:139], v[52:55]\n"
    "ds_read_b64_tr_b16 v[154:155], v201 offset:29184\n"
    "v_xor_b32 v253, 0x40, v211\n"
    "v_add_u32 v253, s67, v253\n"
    "v_mfma_f32_16x16x32_bf16 v[60:63], v[172:175], v[140:143], v[60:63]\n"
    "ds_read_b64_tr_b16 v[156:157], v202 offset:28672\n"
    "v_cvt_pk_bf16_f32 v86, v44, v45\n"
    "v_mfma_f32_16x16x32_bf16 v[56:59], v[168:171], v[140:143], v[56:59]\n"
    "ds_read_b64_tr_b16 v[158:159], v202 offset:29184\n"
    "v_cvt_pk_bf16_f32 v87, v46, v47\n"
    "v_mfma_f32_16x16x32_bf16 v[12:15], v[116:119], v[92:95], v[12:15]\n"
    "ds_read_b128 v[168:171], v253 offset:0\n"
    "ds_read_b128 v[172:175], v253 offset:4096\n"
    "v_mfma_f32_16x16x32_bf16 v[4:7], v[116:119], v[88:91], v[4:7]\n"
    "v_cvt_pk_bf16_f32 v84, v40, v41\n"
    "s_waitcnt lgkmcnt(8)\n"
    "v_mfma_f32_16x16x32_bf16 v[48:51], v[176:179], v[144:147], v[48:51]\n"
    "s_barrier\n"
    "ds_read_b64_tr_b16 v[128:129], v203 offset:16384\n"
    "v_cvt_pk_bf16_f32 v85, v42, v43\n"
    "v_mfma_f32_16x16x32_bf16 v[52:55], v[180:183], v[144:147], v[52:55]\n"
    "ds_read_b64_tr_b16 v[130:131], v203 offset:16896\n"
    "v_xor_b32 v252, 0x80, v211\n"
    "v_add_u32 v252, s67, v252\n"
    "v_mfma_f32_16x16x32_bf16 v[60:63], v[180:183], v[148:151], v[60:63]\n"
    "ds_read_b64_tr_b16 v[132:133], v204 offset:16384\n"
    "v_mfma_f32_16x16x32_bf16 v[56:59], v[176:179], v[148:151], v[56:59]\n"
    "ds_read_b64_tr_b16 v[134:135], v204 offset:16896\n"
    "ds_read_b128 v[176:179], v252 offset:0\n"
    "v_mfma_f32_16x16x32_bf16 v[16:19], v[120:123], v[88:91], v[16:19]\n"
    "ds_read_b128 v[180:183], v252 offset:4096\n"
    "v_mfma_f32_16x16x32_bf16 v[24:27], v[120:123], v[92:95], v[24:27]\n"
    "s_waitcnt lgkmcnt(8)\n"
    "v_mfma_f32_16x16x32_bf16 v[48:51], v[184:187], v[152:155], v[48:51]\n"
    "ds_read_b64_tr_b16 v[136:137], v203 offset:20480\n"
    "ds_read_b128 v[96:99], v207 offset:32768\n"
    "v_mfma_f32_16x16x32_bf16 v[52:55], v[188:191], v[152:155], v[52:55]\n"
    "ds_read_b64_tr_b16 v[138:139], v203 offset:20992\n"
    "ds_read_b128 v[100:103], v207 offset:33792\n"
    "v_mfma_f32_16x16x32_bf16 v[60:63], v[188:191], v[156:159], v[60:63]\n"
    "v_xor_b32 v253, 0xc0, v211\n"
    "v_add_u32 v253, s67, v253\n"
    "ds_read_b64_tr_b16 v[140:141], v204 offset:20480\n"
    "v_mfma_f32_16x16x32_bf16 v[56:59], v[184:187], v[156:159], v[56:59]\n"
    "ds_read_b64_tr_b16 v[142:143], v204 offset:20992\n"
    "ds_read_b128 v[184:187], v253 offset:0\n"
    "v_mfma_f32_16x16x32_bf16 v[28:31], v[124:127], v[92:95], v[28:31]\n"
    "s_waitcnt lgkmcnt(14)\n"
    "ds_read_b128 v[188:191], v253 offset:4096\n"
    "v_mfma_f32_16x16x32_bf16 v[20:23], v[124:127], v[88:91], v[20:23]\n"
    "s_waitcnt lgkmcnt(10)\n"
    "v_mfma_f32_16x16x32_bf16 v[32:35], v[160:163], v[128:131], 0\n"
    "ds_read_b64_tr_b16 v[144:145], v203 offset:24576\n"
    "v_mfma_f32_16x16x32_bf16 v[36:39], v[164:167], v[128:131], 0\n"
    "ds_read_b64_tr_b16 v[146:147], v203 offset:25088\n"
    "v_mfma_f32_16x16x32_bf16 v[44:47], v[164:167], v[132:135], 0\n"
    "ds_read_b64_tr_b16 v[148:149], v204 offset:24576\n"
    "v_cvt_pk_bf16_f32 v88, v48, v49\n"
    "v_mfma_f32_16x16x32_bf16 v[40:43], v[160:163], v[132:135], 0\n"
    "ds_read_b64_tr_b16 v[150:151], v204 offset:25088\n"
    "v_cvt_pk_bf16_f32 v89, v50, v51\n"
    "s_waitcnt lgkmcnt(8)\n"
    "v_mfma_f32_16x16x32_bf16 v[0:3], v[192:195], v[96:99], v[0:3]\n"
    "v_mfma_f32_16x16x32_bf16 v[8:11], v[192:195], v[100:103], v[8:11]\n"
    "v_cvt_pk_bf16_f32 v90, v52, v53\n"
    "s_waitcnt lgkmcnt(6)\n"
    "v_mfma_f32_16x16x32_bf16 v[32:35], v[168:171], v[136:139], v[32:35]\n"
    "ds_read_b64_tr_b16 v[152:153], v203 offset:28672\n"
    "v_cvt_pk_bf16_f32 v91, v54, v55\n"
    "v_mfma_f32_16x16x32_bf16 v[36:39], v[172:175], v[136:139], v[36:39]\n"
    "ds_read_b64_tr_b16 v[154:155], v203 offset:29184\n"
    "v_mfma_f32_16x16x32_bf16 v[44:47], v[172:175], v[140:143], v[44:47]\n"
    "ds_read_b64_tr_b16 v[156:157], v204 offset:28672\n"
    "v_cvt_pk_bf16_f32 v94, v60, v61\n"
    "v_mfma_f32_16x16x32_bf16 v[40:43], v[168:171], v[140:143], v[40:43]\n"
    "ds_read_b64_tr_b16 v[158:159], v204 offset:29184\n"
    "v_cvt_pk_bf16_f32 v95, v62, v63\n"
    "v_mfma_f32_16x16x32_bf16 v[12:15], v[196:199], v[100:103], v[12:15]\n"
    "v_mfma_f32_16x16x32_bf16 v[4:7], v[196:199], v[96:99], v[4:7]\n"
    "v_cvt_pk_bf16_f32 v92, v56, v57\n"
    "s_waitcnt lgkmcnt(4)\n"
    "v_mfma_f32_16x16x32_bf16 v[32:35], v[176:179], v[144:147], v[32:35]\n"
    "ds_read_b64_tr_b16 v[128:129], v205 offset:16384\n"
    "v_cvt_pk_bf16_f32 v93, v58, v59\n"
    "v_mfma_f32_16x16x32_bf16 v[36:39], v[180:183], v[144:147], v[36:39]\n"
    "ds_read_b64_tr_b16 v[130:131], v205 offset:16896\n"
    "v_mfma_f32_16x16x32_bf16 v[44:47], v[180:183], v[148:151], v[44:47]\n"
    "ds_read_b64_tr_b16 v[132:133], v206 offset:16384\n"
    "v_mfma_f32_16x16x32_bf16 v[40:43], v[176:179], v[148:151], v[40:43]\n"
    "ds_read_b64_tr_b16 v[134:135], v206 offset:16896\n"
    "v_mfma_f32_16x16x32_bf16 v[16:19], v[220:223], v[96:99], v[16:19]\n"
    "v_mfma_f32_16x16x32_bf16 v[24:27], v[220:223], v[100:103], v[24:27]\n"
    "s_waitcnt lgkmcnt(4)\n"
    "v_mfma_f32_16x16x32_bf16 v[32:35], v[184:187], v[152:155], v[32:35]\n"
    "ds_read_b64_tr_b16 v[136:137], v205 offset:20480\n"
    "ds_read_b128 v[104:107], v207 offset:40960\n"
    "v_mfma_f32_16x16x32_bf16 v[36:39], v[188:191], v[152:155], v[36:39]\n"
    "ds_read_b64_tr_b16 v[138:139], v205 offset:20992\n"
    "ds_read_b128 v[108:111], v207 offset:41984\n"
    "v_mfma_f32_16x16x32_bf16 v[44:47], v[188:191], v[156:159], v[44:47]\n"
    "ds_read_b64_tr_b16 v[140:141], v206 offset:20480\n"
    "v_mfma_f32_16x16x32_bf16 v[40:43], v[184:187], v[156:159], v[40:43]\n"
    "ds_read_b64_tr_b16 v[142:143], v206 offset:20992\n"
    "v_mfma_f32_16x16x32_bf16 v[28:31], v[224:227], v[100:103], v[28:31]\n"
    "v_mfma_f32_16x16x32_bf16 v[20:23], v[224:227], v[96:99], v[20:23]\n"
    "s_waitcnt lgkmcnt(6)\n"
    "v_mfma_f32_16x16x32_bf16 v[48:51], v[160:163], v[128:131], 0\n"
    "ds_read_b64_tr_b16 v[144:145], v205 offset:24576\n"
    "v_mfma_f32_16x16x32_bf16 v[52:55], v[164:167], v[128:131], 0\n"
    "ds_read_b64_tr_b16 v[146:147], v205 offset:25088\n"
    "v_mfma_f32_16x16x32_bf16 v[60:63], v[164:167], v[132:135], 0\n"
    "ds_read_b64_tr_b16 v[148:149], v206 offset:24576\n"
    "v_cvt_pk_bf16_f32 v96, v32, v33\n"
    "v_mfma_f32_16x16x32_bf16 v[56:59], v[160:163], v[132:135], 0\n"
    "ds_read_b64_tr_b16 v[150:151], v206 offset:25088\n"
    "v_cvt_pk_bf16_f32 v97, v34, v35\n"
    "s_waitcnt lgkmcnt(6)\n"
    "v_mfma_f32_16x16x32_bf16 v[0:3], v[228:231], v[104:107], v[0:3]\n"
    "v_mfma_f32_16x16x32_bf16 v[8:11], v[228:231], v[108:111], v[8:11]\n"
    "v_cvt_pk_bf16_f32 v98, v36, v37\n"
    "s_waitcnt lgkmcnt(4)\n"
    "v_mfma_f32_16x16x32_bf16 v[48:51], v[168:171], v[136:139], v[48:51]\n"
    "ds_read_b64_tr_b16 v[152:153], v205 offset:28672\n"
    "v_cvt_pk_bf16_f32 v99, v38, v39\n"
    "v_mfma_f32_16x16x32_bf16 v[52:55], v[172:175], v[136:139], v[52:55]\n"
    "ds_read_b64_tr_b16 v[154:155], v205 offset:29184\n"
    "v_mfma_f32_16x16x32_bf16 v[60:63], v[172:175], v[140:143], v[60:63]\n"
    "ds_read_b64_tr_b16 v[156:157], v206 offset:28672\n"
    "v_cvt_pk_bf16_f32 v102, v44, v45\n"
    "v_mfma_f32_16x16x32_bf16 v[56:59], v[168:171], v[140:143], v[56:59]\n"
    "ds_read_b64_tr_b16 v[158:159], v206 offset:29184\n"
    "v_cvt_pk_bf16_f32 v103, v46, v47\n"
    "v_mfma_f32_16x16x32_bf16 v[12:15], v[232:235], v[108:111], v[12:15]\n"
    "v_mfma_f32_16x16x32_bf16 v[4:7], v[232:235], v[104:107], v[4:7]\n"
    "v_cvt_pk_bf16_f32 v100, v40, v41\n"
    "s_waitcnt lgkmcnt(4)\n"
    "v_mfma_f32_16x16x32_bf16 v[48:51], v[176:179], v[144:147], v[48:51]\n"
    "s_barrier\n"
    "ds_read_b64_tr_b16 v[128:129], v203 offset:49152\n"
    "v_cvt_pk_bf16_f32 v101, v42, v43\n"
    "v_mfma_f32_16x16x32_bf16 v[52:55], v[180:183], v[144:147], v[52:55]\n"
    "ds_read_b64_tr_b16 v[130:131], v203 offset:49664\n"
    "v_mfma_f32_16x16x32_bf16 v[60:63], v[180:183], v[148:151], v[60:63]\n"
    "ds_read_b64_tr_b16 v[132:133], v204 offset:49152\n"
    "v_mfma_f32_16x16x32_bf16 v[56:59], v[176:179], v[148:151], v[56:59]\n"
    "ds_read_b64_tr_b16 v[134:135], v204 offset:49664\n"
    "v_mfma_f32_16x16x32_bf16 v[16:19], v[244:247], v[104:107], v[16:19]\n"
    "v_mfma_f32_16x16x32_bf16 v[24:27], v[244:247], v[108:111], v[24:27]\n"
    "s_waitcnt lgkmcnt(4)\n"
    "v_mfma_f32_16x16x32_bf16 v[48:51], v[184:187], v[152:155], v[48:51]\n"
    "ds_read_b64_tr_b16 v[136:137], v203 offset:53248\n"
    "ds_read_b128 v[112:115], v219 offset:0\n"
    "v_mfma_f32_16x16x32_bf16 v[52:55], v[188:191], v[152:155], v[52:55]\n"
    "ds_read_b64_tr_b16 v[138:139], v203 offset:53760\n"
    "ds_read_b128 v[116:119], v219 offset:1024\n"
    "v_mfma_f32_16x16x32_bf16 v[60:63], v[188:191], v[156:159], v[60:63]\n"
    "ds_read_b64_tr_b16 v[140:141], v204 offset:53248\n"
    "v_mfma_f32_16x16x32_bf16 v[56:59], v[184:187], v[156:159], v[56:59]\n"
    "ds_read_b64_tr_b16 v[142:143], v204 offset:53760\n"
    "v_mfma_f32_16x16x32_bf16 v[28:31], v[248:251], v[108:111], v[28:31]\n"
    "v_mfma_f32_16x16x32_bf16 v[20:23], v[248:251], v[104:107], v[20:23]\n"
    "s_waitcnt lgkmcnt(6)\n"
    "v_mfma_f32_16x16x32_bf16 v[32:35], v[160:163], v[128:131], 0\n"
    "ds_read_b64_tr_b16 v[144:145], v203 offset:57344\n"
    "v_mfma_f32_16x16x32_bf16 v[36:39], v[164:167], v[128:131], 0\n"
    "ds_read_b64_tr_b16 v[146:147], v203 offset:57856\n"
    "v_mfma_f32_16x16x32_bf16 v[44:47], v[164:167], v[132:135], 0\n"
    "ds_read_b64_tr_b16 v[148:149], v204 offset:57344\n"
    "v_cvt_pk_bf16_f32 v104, v48, v49\n"
    "v_mfma_f32_16x16x32_bf16 v[40:43], v[160:163], v[132:135], 0\n"
    "ds_read_b64_tr_b16 v[150:151], v204 offset:57856\n"
    "v_cvt_pk_bf16_f32 v105, v50, v51\n"
    "s_waitcnt lgkmcnt(6)\n"
    "v_mfma_f32_16x16x32_bf16 v[0:3], v[72:75], v[112:115], v[0:3]\n"
    "v_mfma_f32_16x16x32_bf16 v[8:11], v[72:75], v[116:119], v[8:11]\n"
    "v_cvt_pk_bf16_f32 v106, v52, v53\n"
    "s_waitcnt lgkmcnt(4)\n"
    "v_mfma_f32_16x16x32_bf16 v[32:35], v[168:171], v[136:139], v[32:35]\n"
    "ds_read_b64_tr_b16 v[152:153], v203 offset:61440\n"
    "v_cvt_pk_bf16_f32 v107, v54, v55\n"
    "v_mfma_f32_16x16x32_bf16 v[36:39], v[172:175], v[136:139], v[36:39]\n"
    "ds_read_b64_tr_b16 v[154:155], v203 offset:61952\n"
    "v_mfma_f32_16x16x32_bf16 v[44:47], v[172:175], v[140:143], v[44:47]\n"
    "ds_read_b64_tr_b16 v[156:157], v204 offset:61440\n"
    "v_cvt_pk_bf16_f32 v110, v60, v61\n"
    "v_mfma_f32_16x16x32_bf16 v[40:43], v[168:171], v[140:143], v[40:43]\n"
    "ds_read_b64_tr_b16 v[158:159], v204 offset:61952\n"
    "v_cvt_pk_bf16_f32 v111, v62, v63\n"
    "v_mfma_f32_16x16x32_bf16 v[12:15], v[76:79], v[116:119], v[12:15]\n"
    "v_mfma_f32_16x16x32_bf16 v[4:7], v[76:79], v[112:115], v[4:7]\n"
    "v_cvt_pk_bf16_f32 v108, v56, v57\n"
    "s_waitcnt lgkmcnt(4)\n"
    "v_mfma_f32_16x16x32_bf16 v[32:35], v[176:179], v[144:147], v[32:35]\n"
    "ds_read_b64_tr_b16 v[128:129], v210 offset:0\n"
    "v_cvt_pk_bf16_f32 v109, v58, v59\n"
    "v_mfma_f32_16x16x32_bf16 v[36:39], v[180:183], v[144:147], v[36:39]\n"
    "ds_read_b64_tr_b16 v[130:131], v210 offset:512\n"
    "v_mfma_f32_16x16x32_bf16 v[44:47], v[180:183], v[148:151], v[44:47]\n"
    "ds_read_b64_tr_b16 v[132:133], v200 offset:0\n"
    "v_mfma_f32_16x16x32_bf16 v[40:43], v[176:179], v[148:151], v[40:43]\n"
    "ds_read_b64_tr_b16 v[134:135], v200 offset:512\n"
    "v_mfma_f32_16x16x32_bf16 v[16:19], v[64:67], v[112:115], v[16:19]\n"
    "v_mfma_f32_16x16x32_bf16 v[24:27], v[64:67], v[116:119], v[24:27]\n"
    "s_waitcnt lgkmcnt(4)\n"
    "v_mfma_f32_16x16x32_bf16 v[32:35], v[184:187], v[152:155], v[32:35]\n"
    "ds_read_b64_tr_b16 v[136:137], v210 offset:4096\n"
    "ds_read_b128 v[72:75], v219 offset:8192\n"
    "v_mfma_f32_16x16x32_bf16 v[36:39], v[188:191], v[152:155], v[36:39]\n"
    "ds_read_b64_tr_b16 v[138:139], v210 offset:4608\n"
    "ds_read_b128 v[76:79], v219 offset:9216\n"
    "v_mfma_f32_16x16x32_bf16 v[44:47], v[188:191], v[156:159], v[44:47]\n"
    "ds_read_b64_tr_b16 v[140:141], v200 offset:4096\n"
    "v_mfma_f32_16x16x32_bf16 v[40:43], v[184:187], v[156:159], v[40:43]\n"
    "ds_read_b64_tr_b16 v[142:143], v200 offset:4608\n"
    "v_mfma_f32_16x16x32_bf16 v[28:31], v[68:71], v[116:119], v[28:31]\n"
    "v_mfma_f32_16x16x32_bf16 v[20:23], v[68:71], v[112:115], v[20:23]\n"
    "s_waitcnt lgkmcnt(6)\n"
    "v_mfma_f32_16x16x32_bf16 v[48:51], v[160:163], v[128:131], 0\n"
    "ds_read_b64_tr_b16 v[144:145], v210 offset:8192\n"
    "v_mfma_f32_16x16x32_bf16 v[52:55], v[164:167], v[128:131], 0\n"
    "ds_read_b64_tr_b16 v[146:147], v210 offset:8704\n"
    "v_mfma_f32_16x16x32_bf16 v[60:63], v[164:167], v[132:135], 0\n"
    "ds_read_b64_tr_b16 v[148:149], v200 offset:8192\n"
    "v_cvt_pk_bf16_f32 v64, v32, v33\n"
    "v_mfma_f32_16x16x32_bf16 v[56:59], v[160:163], v[132:135], 0\n"
    "ds_read_b64_tr_b16 v[150:151], v200 offset:8704\n"
    "v_cvt_pk_bf16_f32 v65, v34, v35\n"
    "s_waitcnt lgkmcnt(6)\n"
    "v_mfma_f32_16x16x32_bf16 v[0:3], v[80:83], v[72:75], v[0:3]\n"
    "v_mfma_f32_16x16x32_bf16 v[8:11], v[80:83], v[76:79], v[8:11]\n"
    "v_cvt_pk_bf16_f32 v66, v36, v37\n"
    "s_waitcnt lgkmcnt(4)\n"
    "v_mfma_f32_16x16x32_bf16 v[48:51], v[168:171], v[136:139], v[48:51]\n"
    "ds_read_b64_tr_b16 v[152:153], v210 offset:12288\n"
    "v_cvt_pk_bf16_f32 v67, v38, v39\n"
    "v_mfma_f32_16x16x32_bf16 v[52:55], v[172:175], v[136:139], v[52:55]\n"
    "ds_read_b64_tr_b16 v[154:155], v210 offset:12800\n"
    "v_mfma_f32_16x16x32_bf16 v[60:63], v[172:175], v[140:143], v[60:63]\n"
    "ds_read_b64_tr_b16 v[156:157], v200 offset:12288\n"
    "v_cvt_pk_bf16_f32 v70, v44, v45\n"
    "v_mfma_f32_16x16x32_bf16 v[56:59], v[168:171], v[140:143], v[56:59]\n"
    "ds_read_b64_tr_b16 v[158:159], v200 offset:12800\n"
    "v_cvt_pk_bf16_f32 v71, v46, v47\n"
    "v_mfma_f32_16x16x32_bf16 v[12:15], v[84:87], v[76:79], v[12:15]\n"
    "v_mfma_f32_16x16x32_bf16 v[4:7], v[84:87], v[72:75], v[4:7]\n"
    "v_cvt_pk_bf16_f32 v68, v40, v41\n"
    "s_waitcnt lgkmcnt(4)\n"
    "v_mfma_f32_16x16x32_bf16 v[48:51], v[176:179], v[144:147], v[48:51]\n"
    "s_barrier\n"
    "ds_read_b64_tr_b16 v[128:129], v210 offset:16384\n"
    "v_cvt_pk_bf16_f32 v69, v42, v43\n"
    "v_mfma_f32_16x16x32_bf16 v[52:55], v[180:183], v[144:147], v[52:55]\n"
    "ds_read_b64_tr_b16 v[130:131], v210 offset:16896\n"
    "v_mfma_f32_16x16x32_bf16 v[60:63], v[180:183], v[148:151], v[60:63]\n"
    "ds_read_b64_tr_b16 v[132:133], v200 offset:16384\n"
    "v_mfma_f32_16x16x32_bf16 v[56:59], v[176:179], v[148:151], v[56:59]\n"
    "ds_read_b64_tr_b16 v[134:135], v200 offset:16896\n"
    "v_mfma_f32_16x16x32_bf16 v[16:19], v[88:91], v[72:75], v[16:19]\n"
    "v_mfma_f32_16x16x32_bf16 v[24:27], v[88:91], v[76:79], v[24:27]\n"
    "s_waitcnt lgkmcnt(4)\n"
    "v_mfma_f32_16x16x32_bf16 v[48:51], v[184:187], v[152:155], v[48:51]\n"
    "ds_read_b64_tr_b16 v[136:137], v210 offset:20480\n"
    "ds_read_b128 v[80:83], v212 offset:0\n"
    "v_mfma_f32_16x16x32_bf16 v[52:55], v[188:191], v[152:155], v[52:55]\n"
    "ds_read_b64_tr_b16 v[138:139], v210 offset:20992\n"
    "ds_read_b128 v[84:87], v212 offset:1024\n"
    "v_mfma_f32_16x16x32_bf16 v[60:63], v[188:191], v[156:159], v[60:63]\n"
    "ds_read_b64_tr_b16 v[140:141], v200 offset:20480\n"
    "v_mfma_f32_16x16x32_bf16 v[56:59], v[184:187], v[156:159], v[56:59]\n"
    "ds_read_b64_tr_b16 v[142:143], v200 offset:20992\n"
    "v_mfma_f32_16x16x32_bf16 v[28:31], v[92:95], v[76:79], v[28:31]\n"
    "v_mfma_f32_16x16x32_bf16 v[20:23], v[92:95], v[72:75], v[20:23]\n"
    "s_waitcnt lgkmcnt(6)\n"
    "v_mfma_f32_16x16x32_bf16 v[32:35], v[160:163], v[128:131], 0\n"
    "ds_read_b64_tr_b16 v[144:145], v210 offset:24576\n"
    "v_mfma_f32_16x16x32_bf16 v[36:39], v[164:167], v[128:131], 0\n"
    "ds_read_b64_tr_b16 v[146:147], v210 offset:25088\n"
    "v_mfma_f32_16x16x32_bf16 v[44:47], v[164:167], v[132:135], 0\n"
    "ds_read_b64_tr_b16 v[148:149], v200 offset:24576\n"
    "v_cvt_pk_bf16_f32 v72, v48, v49\n"
    "v_mfma_f32_16x16x32_bf16 v[40:43], v[160:163], v[132:135], 0\n"
    "ds_read_b64_tr_b16 v[150:151], v200 offset:25088\n"
    "v_cvt_pk_bf16_f32 v73, v50, v51\n"
    "s_waitcnt lgkmcnt(6)\n"
    "v_mfma_f32_16x16x32_bf16 v[0:3], v[96:99], v[80:83], v[0:3]\n"
    "v_mfma_f32_16x16x32_bf16 v[8:11], v[96:99], v[84:87], v[8:11]\n"
    "v_cvt_pk_bf16_f32 v74, v52, v53\n"
    "s_waitcnt lgkmcnt(4)\n"
    "v_mfma_f32_16x16x32_bf16 v[32:35], v[168:171], v[136:139], v[32:35]\n"
    "ds_read_b64_tr_b16 v[152:153], v210 offset:28672\n"
    "v_cvt_pk_bf16_f32 v75, v54, v55\n"
    "v_mfma_f32_16x16x32_bf16 v[36:39], v[172:175], v[136:139], v[36:39]\n"
    "ds_read_b64_tr_b16 v[154:155], v210 offset:29184\n"
    "v_mfma_f32_16x16x32_bf16 v[44:47], v[172:175], v[140:143], v[44:47]\n"
    "ds_read_b64_tr_b16 v[156:157], v200 offset:28672\n"
    "v_cvt_pk_bf16_f32 v78, v60, v61\n"
    "v_mfma_f32_16x16x32_bf16 v[40:43], v[168:171], v[140:143], v[40:43]\n"
    "ds_read_b64_tr_b16 v[158:159], v200 offset:29184\n"
    "v_cvt_pk_bf16_f32 v79, v62, v63\n"
    "v_mfma_f32_16x16x32_bf16 v[12:15], v[100:103], v[84:87], v[12:15]\n"
    "v_mfma_f32_16x16x32_bf16 v[4:7], v[100:103], v[80:83], v[4:7]\n"
    "v_cvt_pk_bf16_f32 v76, v56, v57\n"
    "s_waitcnt lgkmcnt(4)\n"
    "v_mfma_f32_16x16x32_bf16 v[32:35], v[176:179], v[144:147], v[32:35]\n"
    "ds_read_b64_tr_b16 v[128:129], v210 offset:32768\n"
    "v_cvt_pk_bf16_f32 v77, v58, v59\n"
    "v_mfma_f32_16x16x32_bf16 v[36:39], v[180:183], v[144:147], v[36:39]\n"
    "ds_read_b64_tr_b16 v[130:131], v210 offset:33280\n"
    "v_mfma_f32_16x16x32_bf16 v[44:47], v[180:183], v[148:151], v[44:47]\n"
    "ds_read_b64_tr_b16 v[132:133], v200 offset:32768\n"
    "v_mfma_f32_16x16x32_bf16 v[40:43], v[176:179], v[148:151], v[40:43]\n"
    "ds_read_b64_tr_b16 v[134:135], v200 offset:33280\n"
    "v_mfma_f32_16x16x32_bf16 v[16:19], v[104:107], v[80:83], v[16:19]\n"
    "v_mfma_f32_16x16x32_bf16 v[24:27], v[104:107], v[84:87], v[24:27]\n"
    "s_waitcnt lgkmcnt(4)\n"
    "v_mfma_f32_16x16x32_bf16 v[32:35], v[184:187], v[152:155], v[32:35]\n"
    "ds_read_b64_tr_b16 v[136:137], v210 offset:36864\n"
    "ds_read_b128 v[88:91], v212 offset:8192\n"
    "v_mfma_f32_16x16x32_bf16 v[36:39], v[188:191], v[152:155], v[36:39]\n"
    "ds_read_b64_tr_b16 v[138:139], v210 offset:37376\n"
    "ds_read_b128 v[92:95], v212 offset:9216\n"
    "v_mfma_f32_16x16x32_bf16 v[44:47], v[188:191], v[156:159], v[44:47]\n"
    "ds_read_b64_tr_b16 v[140:141], v200 offset:36864\n"
    "v_mfma_f32_16x16x32_bf16 v[40:43], v[184:187], v[156:159], v[40:43]\n"
    "ds_read_b64_tr_b16 v[142:143], v200 offset:37376\n"
    "v_mfma_f32_16x16x32_bf16 v[28:31], v[108:111], v[84:87], v[28:31]\n"
    "v_mfma_f32_16x16x32_bf16 v[20:23], v[108:111], v[80:83], v[20:23]\n"
    "s_waitcnt lgkmcnt(6)\n"
    "v_mfma_f32_16x16x32_bf16 v[48:51], v[160:163], v[128:131], 0\n"
    "ds_read_b64_tr_b16 v[144:145], v210 offset:40960\n"
    "v_mfma_f32_16x16x32_bf16 v[52:55], v[164:167], v[128:131], 0\n"
    "ds_read_b64_tr_b16 v[146:147], v210 offset:41472\n"
    "ds_read_b128 v[96:99], v212 offset:16384\n"
    "v_mfma_f32_16x16x32_bf16 v[60:63], v[164:167], v[132:135], 0\n"
    "ds_read_b64_tr_b16 v[148:149], v200 offset:40960\n"
    "v_cvt_pk_bf16_f32 v80, v32, v33\n"
    "v_mfma_f32_16x16x32_bf16 v[56:59], v[160:163], v[132:135], 0\n"
    "ds_read_b128 v[100:103], v212 offset:17408\n"
    "ds_read_b64_tr_b16 v[150:151], v200 offset:41472\n"
    "s_waitcnt lgkmcnt(8)\n"
    "v_mfma_f32_16x16x32_bf16 v[0:3], v[64:67], v[88:91], v[0:3]\n"
    "v_cvt_pk_bf16_f32 v81, v34, v35\n"
    "v_mfma_f32_16x16x32_bf16 v[8:11], v[64:67], v[92:95], v[8:11]\n"
    "v_cvt_pk_bf16_f32 v82, v36, v37\n"
    "s_waitcnt lgkmcnt(6)\n"
    "v_mfma_f32_16x16x32_bf16 v[48:51], v[168:171], v[136:139], v[48:51]\n"
    "ds_read_b64_tr_b16 v[152:153], v210 offset:45056\n"
    "v_cvt_pk_bf16_f32 v83, v38, v39\n"
    "v_mfma_f32_16x16x32_bf16 v[52:55], v[172:175], v[136:139], v[52:55]\n"
    "ds_read_b64_tr_b16 v[154:155], v210 offset:45568\n"
    "v_mfma_f32_16x16x32_bf16 v[60:63], v[172:175], v[140:143], v[60:63]\n"
    "ds_read_b64_tr_b16 v[156:157], v200 offset:45056\n"
    "v_cvt_pk_bf16_f32 v86, v44, v45\n"
    "v_mfma_f32_16x16x32_bf16 v[56:59], v[168:171], v[140:143], v[56:59]\n"
    "ds_read_b64_tr_b16 v[158:159], v200 offset:45568\n"
    "v_cvt_pk_bf16_f32 v87, v46, v47\n"
    "v_mfma_f32_16x16x32_bf16 v[12:15], v[68:71], v[92:95], v[12:15]\n"
    "v_mfma_f32_16x16x32_bf16 v[4:7], v[68:71], v[88:91], v[4:7]\n"
    "v_cvt_pk_bf16_f32 v84, v40, v41\n"
    "s_waitcnt lgkmcnt(4)\n"
    "v_mfma_f32_16x16x32_bf16 v[48:51], v[176:179], v[144:147], v[48:51]\n"
    "s_barrier\n"
    "ds_read_b64_tr_b16 v[128:129], v203 offset:0\n"
    "v_cvt_pk_bf16_f32 v85, v42, v43\n"
    "v_mfma_f32_16x16x32_bf16 v[52:55], v[180:183], v[144:147], v[52:55]\n"
    "ds_read_b64_tr_b16 v[130:131], v203 offset:512\n"
    "v_mfma_f32_16x16x32_bf16 v[60:63], v[180:183], v[148:151], v[60:63]\n"
    "ds_read_b64_tr_b16 v[132:133], v204 offset:0\n"
    "v_mfma_f32_16x16x32_bf16 v[56:59], v[176:179], v[148:151], v[56:59]\n"
    "ds_read_b64_tr_b16 v[134:135], v204 offset:512\n"
    "v_mfma_f32_16x16x32_bf16 v[0:3], v[72:75], v[96:99], v[0:3]\n"
    "v_mfma_f32_16x16x32_bf16 v[8:11], v[72:75], v[100:103], v[8:11]\n"
    "s_waitcnt lgkmcnt(4)\n"
    "v_mfma_f32_16x16x32_bf16 v[48:51], v[184:187], v[152:155], v[48:51]\n"
    "ds_read_b64_tr_b16 v[136:137], v203 offset:4096\n"
    "ds_read_b128 v[64:67], v212 offset:24576\n"
    "v_mfma_f32_16x16x32_bf16 v[52:55], v[188:191], v[152:155], v[52:55]\n"
    "ds_read_b64_tr_b16 v[138:139], v203 offset:4608\n"
    "ds_read_b128 v[68:71], v212 offset:25600\n"
    "v_mfma_f32_16x16x32_bf16 v[60:63], v[188:191], v[156:159], v[60:63]\n"
    "ds_read_b64_tr_b16 v[140:141], v204 offset:4096\n"
    "v_mfma_f32_16x16x32_bf16 v[56:59], v[184:187], v[156:159], v[56:59]\n"
    "ds_read_b64_tr_b16 v[142:143], v204 offset:4608\n"
    "v_mfma_f32_16x16x32_bf16 v[12:15], v[76:79], v[100:103], v[12:15]\n"
    "v_mfma_f32_16x16x32_bf16 v[4:7], v[76:79], v[96:99], v[4:7]\n"
    "s_waitcnt lgkmcnt(6)\n"
    "v_mfma_f32_16x16x32_bf16 v[32:35], v[160:163], v[128:131], 0\n"
    "ds_read_b64_tr_b16 v[144:145], v203 offset:8192\n"
    "v_mfma_f32_16x16x32_bf16 v[36:39], v[164:167], v[128:131], 0\n"
    "ds_read_b64_tr_b16 v[146:147], v203 offset:8704\n"
    "v_mfma_f32_16x16x32_bf16 v[44:47], v[164:167], v[132:135], 0\n"
    "ds_read_b64_tr_b16 v[148:149], v204 offset:8192\n"
    "v_cvt_pk_bf16_f32 v72, v48, v49\n"
    "v_mfma_f32_16x16x32_bf16 v[40:43], v[160:163], v[132:135], 0\n"
    "ds_read_b64_tr_b16 v[150:151], v204 offset:8704\n"
    "v_cvt_pk_bf16_f32 v73, v50, v51\n"
    "s_waitcnt lgkmcnt(6)\n"
    "v_mfma_f32_16x16x32_bf16 v[0:3], v[80:83], v[64:67], v[0:3]\n"
    "s_waitcnt lgkmcnt(4)\n"
    "v_mfma_f32_16x16x32_bf16 v[32:35], v[168:171], v[136:139], v[32:35]\n"
    "ds_read_b64_tr_b16 v[152:153], v203 offset:12288\n"
    "v_cvt_pk_bf16_f32 v74, v52, v53\n"
    "v_mfma_f32_16x16x32_bf16 v[36:39], v[172:175], v[136:139], v[36:39]\n"
    "ds_read_b64_tr_b16 v[154:155], v203 offset:12800\n"
    "v_cvt_pk_bf16_f32 v75, v54, v55\n"
    "v_mfma_f32_16x16x32_bf16 v[44:47], v[172:175], v[140:143], v[44:47]\n"
    "ds_read_b64_tr_b16 v[156:157], v204 offset:12288\n"
    "v_mfma_f32_16x16x32_bf16 v[40:43], v[168:171], v[140:143], v[40:43]\n"
    "ds_read_b64_tr_b16 v[158:159], v204 offset:12800\n"
    "v_cvt_pk_bf16_f32 v78, v60, v61\n"
    "v_mfma_f32_16x16x32_bf16 v[8:11], v[80:83], v[68:71], v[8:11]\n"
    "v_cvt_pk_bf16_f32 v79, v62, v63\n"
    "s_waitcnt lgkmcnt(4)\n"
    "v_mfma_f32_16x16x32_bf16 v[32:35], v[176:179], v[144:147], v[32:35]\n"
    "ds_read_b64_tr_b16 v[128:129], v203 offset:16384\n"
    "v_mfma_f32_16x16x32_bf16 v[36:39], v[180:183], v[144:147], v[36:39]\n"
    "ds_read_b64_tr_b16 v[130:131], v203 offset:16896\n"
    "v_cvt_pk_bf16_f32 v76, v56, v57\n"
    "v_mfma_f32_16x16x32_bf16 v[44:47], v[180:183], v[148:151], v[44:47]\n"
    "ds_read_b64_tr_b16 v[132:133], v204 offset:16384\n"
    "v_cvt_pk_bf16_f32 v77, v58, v59\n"
    "v_mfma_f32_16x16x32_bf16 v[40:43], v[176:179], v[148:151], v[40:43]\n"
    "ds_read_b64_tr_b16 v[134:135], v204 offset:16896\n"
    "v_mfma_f32_16x16x32_bf16 v[12:15], v[84:87], v[68:71], v[12:15]\n"
    "s_waitcnt lgkmcnt(4)\n"
    "v_mfma_f32_16x16x32_bf16 v[32:35], v[184:187], v[152:155], v[32:35]\n"
    "ds_read_b64_tr_b16 v[136:137], v203 offset:20480\n"
    "ds_read_b128 v[104:107], v207 offset:16384\n"
    "v_mfma_f32_16x16x32_bf16 v[36:39], v[188:191], v[152:155], v[36:39]\n"
    "ds_read_b64_tr_b16 v[138:139], v203 offset:20992\n"
    "ds_read_b128 v[108:111], v207 offset:17408\n"
    "v_mfma_f32_16x16x32_bf16 v[44:47], v[188:191], v[156:159], v[44:47]\n"
    "ds_read_b64_tr_b16 v[140:141], v204 offset:20480\n"
    "v_mfma_f32_16x16x32_bf16 v[40:43], v[184:187], v[156:159], v[40:43]\n"
    "ds_read_b64_tr_b16 v[142:143], v204 offset:20992\n"
    "v_mfma_f32_16x16x32_bf16 v[4:7], v[84:87], v[64:67], v[4:7]\n"
    "s_waitcnt lgkmcnt(6)\n"
    "v_mfma_f32_16x16x32_bf16 v[48:51], v[160:163], v[128:131], 0\n"
    "ds_read_b64_tr_b16 v[144:145], v203 offset:24576\n"
    "v_mfma_f32_16x16x32_bf16 v[52:55], v[164:167], v[128:131], 0\n"
    "ds_read_b64_tr_b16 v[146:147], v203 offset:25088\n"
    "v_mfma_f32_16x16x32_bf16 v[60:63], v[164:167], v[132:135], 0\n"
    "ds_read_b64_tr_b16 v[148:149], v204 offset:24576\n"
    "v_cvt_pk_bf16_f32 v80, v32, v33\n"
    "v_mfma_f32_16x16x32_bf16 v[56:59], v[160:163], v[132:135], 0\n"
    "ds_read_b64_tr_b16 v[150:151], v204 offset:25088\n"
    "v_cvt_pk_bf16_f32 v81, v34, v35\n"
    "s_waitcnt lgkmcnt(6)\n"
    "v_mfma_f32_16x16x32_bf16 v[0:3], v[72:75], v[104:107], v[0:3]\n"
    "s_waitcnt lgkmcnt(4)\n"
    "v_mfma_f32_16x16x32_bf16 v[48:51], v[168:171], v[136:139], v[48:51]\n"
    "ds_read_b64_tr_b16 v[152:153], v203 offset:28672\n"
    "v_cvt_pk_bf16_f32 v82, v36, v37\n"
    "v_mfma_f32_16x16x32_bf16 v[52:55], v[172:175], v[136:139], v[52:55]\n"
    "ds_read_b64_tr_b16 v[154:155], v203 offset:29184\n"
    "v_cvt_pk_bf16_f32 v83, v38, v39\n"
    "v_mfma_f32_16x16x32_bf16 v[60:63], v[172:175], v[140:143], v[60:63]\n"
    "ds_read_b64_tr_b16 v[156:157], v204 offset:28672\n"
    "v_mfma_f32_16x16x32_bf16 v[56:59], v[168:171], v[140:143], v[56:59]\n"
    "ds_read_b64_tr_b16 v[158:159], v204 offset:29184\n"
    "v_cvt_pk_bf16_f32 v86, v44, v45\n"
    "v_mfma_f32_16x16x32_bf16 v[8:11], v[72:75], v[108:111], v[8:11]\n"
    "v_cvt_pk_bf16_f32 v87, v46, v47\n"
    "s_waitcnt lgkmcnt(4)\n"
    "v_mfma_f32_16x16x32_bf16 v[48:51], v[176:179], v[144:147], v[48:51]\n"
    "s_barrier\n"
    "ds_read_b64_tr_b16 v[128:129], v203 offset:32768\n"
    "v_mfma_f32_16x16x32_bf16 v[52:55], v[180:183], v[144:147], v[52:55]\n"
    "ds_read_b64_tr_b16 v[130:131], v203 offset:33280\n"
    "v_cvt_pk_bf16_f32 v84, v40, v41\n"
    "v_mfma_f32_16x16x32_bf16 v[60:63], v[180:183], v[148:151], v[60:63]\n"
    "ds_read_b64_tr_b16 v[132:133], v204 offset:32768\n"
    "v_cvt_pk_bf16_f32 v85, v42, v43\n"
    "v_mfma_f32_16x16x32_bf16 v[56:59], v[176:179], v[148:151], v[56:59]\n"
    "ds_read_b64_tr_b16 v[134:135], v204 offset:33280\n"
    "v_mfma_f32_16x16x32_bf16 v[12:15], v[76:79], v[108:111], v[12:15]\n"
    "s_waitcnt lgkmcnt(4)\n"
    "v_mfma_f32_16x16x32_bf16 v[48:51], v[184:187], v[152:155], v[48:51]\n"
    "ds_read_b64_tr_b16 v[136:137], v203 offset:36864\n"
    "ds_read_b128 v[112:115], v207 offset:24576\n"
    "v_mfma_f32_16x16x32_bf16 v[52:55], v[188:191], v[152:155], v[52:55]\n"
    "ds_read_b64_tr_b16 v[138:139], v203 offset:37376\n"
    "ds_read_b128 v[116:119], v207 offset:25600\n"
    "v_mfma_f32_16x16x32_bf16 v[60:63], v[188:191], v[156:159], v[60:63]\n"
    "ds_read_b64_tr_b16 v[140:141], v204 offset:36864\n"
    "v_mfma_f32_16x16x32_bf16 v[56:59], v[184:187], v[156:159], v[56:59]\n"
    "ds_read_b64_tr_b16 v[142:143], v204 offset:37376\n"
    "v_mfma_f32_16x16x32_bf16 v[4:7], v[76:79], v[104:107], v[4:7]\n"
    "s_waitcnt lgkmcnt(6)\n"
    "v_mfma_f32_16x16x32_bf16 v[32:35], v[160:163], v[128:131], 0\n"
    "ds_read_b64_tr_b16 v[144:145], v203 offset:40960\n"
    "v_mfma_f32_16x16x32_bf16 v[36:39], v[164:167], v[128:131], 0\n"
    "ds_read_b64_tr_b16 v[146:147], v203 offset:41472\n"
    "v_mfma_f32_16x16x32_bf16 v[44:47], v[164:167], v[132:135], 0\n"
    "ds_read_b64_tr_b16 v[148:149], v204 offset:40960\n"
    "v_cvt_pk_bf16_f32 v72, v48, v49\n"
    "v_mfma_f32_16x16x32_bf16 v[40:43], v[160:163], v[132:135], 0\n"
    "ds_read_b64_tr_b16 v[150:151], v204 offset:41472\n"
    "v_cvt_pk_bf16_f32 v73, v50, v51\n"
    "s_waitcnt lgkmcnt(6)\n"
    "v_mfma_f32_16x16x32_bf16 v[0:3], v[80:83], v[112:115], v[0:3]\n"
    "s_waitcnt lgkmcnt(4)\n"
    "v_mfma_f32_16x16x32_bf16 v[32:35], v[168:171], v[136:139], v[32:35]\n"
    "ds_read_b64_tr_b16 v[152:153], v203 offset:45056\n"
    "v_cvt_pk_bf16_f32 v74, v52, v53\n"
    "v_mfma_f32_16x16x32_bf16 v[36:39], v[172:175], v[136:139], v[36:39]\n"
    "ds_read_b64_tr_b16 v[154:155], v203 offset:45568\n"
    "v_cvt_pk_bf16_f32 v75, v54, v55\n"
    "v_mfma_f32_16x16x32_bf16 v[44:47], v[172:175], v[140:143], v[44:47]\n"
    "ds_read_b64_tr_b16 v[156:157], v204 offset:45056\n"
    "v_mfma_f32_16x16x32_bf16 v[40:43], v[168:171], v[140:143], v[40:43]\n"
    "ds_read_b64_tr_b16 v[158:159], v204 offset:45568\n"
    "v_cvt_pk_bf16_f32 v78, v60, v61\n"
    "v_mfma_f32_16x16x32_bf16 v[8:11], v[80:83], v[116:119], v[8:11]\n"
    "v_cvt_pk_bf16_f32 v79, v62, v63\n"
    "s_waitcnt lgkmcnt(4)\n"
    "v_mfma_f32_16x16x32_bf16 v[32:35], v[176:179], v[144:147], v[32:35]\n"
    "ds_read_b64_tr_b16 v[128:129], v205 offset:49152\n"
    "v_mfma_f32_16x16x32_bf16 v[36:39], v[180:183], v[144:147], v[36:39]\n"
    "ds_read_b64_tr_b16 v[130:131], v205 offset:49664\n"
    "v_cvt_pk_bf16_f32 v76, v56, v57\n"
    "v_mfma_f32_16x16x32_bf16 v[44:47], v[180:183], v[148:151], v[44:47]\n"
    "ds_read_b64_tr_b16 v[132:133], v206 offset:49152\n"
    "v_cvt_pk_bf16_f32 v77, v58, v59\n"
    "v_mfma_f32_16x16x32_bf16 v[40:43], v[176:179], v[148:151], v[40:43]\n"
    "ds_read_b64_tr_b16 v[134:135], v206 offset:49664\n"
    "v_mfma_f32_16x16x32_bf16 v[12:15], v[84:87], v[116:119], v[12:15]\n"
    "s_waitcnt lgkmcnt(4)\n"
    "v_mfma_f32_16x16x32_bf16 v[32:35], v[184:187], v[152:155], v[32:35]\n"
    "ds_read_b64_tr_b16 v[136:137], v205 offset:53248\n"
    "ds_read_b128 v[120:123], v212 offset:0\n"
    "v_mfma_f32_16x16x32_bf16 v[36:39], v[188:191], v[152:155], v[36:39]\n"
    "ds_read_b64_tr_b16 v[138:139], v205 offset:53760\n"
    "ds_read_b128 v[124:127], v212 offset:1024\n"
    "v_mfma_f32_16x16x32_bf16 v[44:47], v[188:191], v[156:159], v[44:47]\n"
    "ds_read_b64_tr_b16 v[140:141], v206 offset:53248\n"
    "v_mfma_f32_16x16x32_bf16 v[40:43], v[184:187], v[156:159], v[40:43]\n"
    "ds_read_b64_tr_b16 v[142:143], v206 offset:53760\n"
    "v_mfma_f32_16x16x32_bf16 v[4:7], v[84:87], v[112:115], v[4:7]\n"
    "s_waitcnt lgkmcnt(6)\n"
    "v_mfma_f32_16x16x32_bf16 v[48:51], v[160:163], v[128:131], 0\n"
    "ds_read_b64_tr_b16 v[144:145], v205 offset:57344\n"
    "v_mfma_f32_16x16x32_bf16 v[52:55], v[164:167], v[128:131], 0\n"
    "ds_read_b64_tr_b16 v[146:147], v205 offset:57856\n"
    "ds_read_b128 v[192:195], v212 offset:8192\n"
    "v_mfma_f32_16x16x32_bf16 v[60:63], v[164:167], v[132:135], 0\n"
    "ds_read_b64_tr_b16 v[148:149], v206 offset:57344\n"
    "v_cvt_pk_bf16_f32 v80, v32, v33\n"
    "v_mfma_f32_16x16x32_bf16 v[56:59], v[160:163], v[132:135], 0\n"
    "ds_read_b128 v[196:199], v212 offset:9216\n"
    "ds_read_b64_tr_b16 v[150:151], v206 offset:57856\n"
    "s_waitcnt lgkmcnt(8)\n"
    "v_mfma_f32_16x16x32_bf16 v[0:3], v[72:75], v[120:123], v[0:3]\n"
    "v_cvt_pk_bf16_f32 v81, v34, v35\n"
    "v_cvt_pk_bf16_f32 v82, v36, v37\n"
    "v_mfma_f32_16x16x32_bf16 v[8:11], v[72:75], v[124:127], v[8:11]\n"
    "v_cvt_pk_bf16_f32 v83, v38, v39\n"
    "s_waitcnt lgkmcnt(6)\n"
    "v_mfma_f32_16x16x32_bf16 v[48:51], v[168:171], v[136:139], v[48:51]\n"
    "ds_read_b64_tr_b16 v[152:153], v205 offset:61440\n"
    "v_cvt_pk_bf16_f32 v86, v44, v45\n"
    "v_mfma_f32_16x16x32_bf16 v[52:55], v[172:175], v[136:139], v[52:55]\n"
    "ds_read_b64_tr_b16 v[154:155], v205 offset:61952\n"
    "v_cvt_pk_bf16_f32 v87, v46, v47\n"
    "v_mfma_f32_16x16x32_bf16 v[60:63], v[172:175], v[140:143], v[60:63]\n"
    "ds_read_b64_tr_b16 v[156:157], v206 offset:61440\n"
    "v_cvt_pk_bf16_f32 v84, v40, v41\n"
    "v_mfma_f32_16x16x32_bf16 v[56:59], v[168:171], v[140:143], v[56:59]\n"
    "ds_read_b64_tr_b16 v[158:159], v206 offset:61952\n"
    "v_cvt_pk_bf16_f32 v85, v42, v43\n"
    "v_mfma_f32_16x16x32_bf16 v[12:15], v[76:79], v[124:127], v[12:15]\n"
    "v_mfma_f32_16x16x32_bf16 v[4:7], v[76:79], v[120:123], v[4:7]\n"
    "s_waitcnt lgkmcnt(4)\n"
    "v_mfma_f32_16x16x32_bf16 v[48:51], v[176:179], v[144:147], v[48:51]\n"
    "ds_read_b64_tr_b16 v[128:129], v201 offset:0\n"
    "v_mfma_f32_16x16x32_bf16 v[52:55], v[180:183], v[144:147], v[52:55]\n"
    "ds_read_b64_tr_b16 v[130:131], v201 offset:512\n"
    "v_mfma_f32_16x16x32_bf16 v[60:63], v[180:183], v[148:151], v[60:63]\n"
    "ds_read_b64_tr_b16 v[132:133], v202 offset:0\n"
    "v_mfma_f32_16x16x32_bf16 v[56:59], v[176:179], v[148:151], v[56:59]\n"
    "ds_read_b64_tr_b16 v[134:135], v202 offset:512\n"
    "v_mfma_f32_16x16x32_bf16 v[0:3], v[80:83], v[192:195], v[0:3]\n"
    "v_mfma_f32_16x16x32_bf16 v[8:11], v[80:83], v[196:199], v[8:11]\n"
    "s_waitcnt lgkmcnt(4)\n"
    "v_mfma_f32_16x16x32_bf16 v[48:51], v[184:187], v[152:155], v[48:51]\n"
    "ds_read_b64_tr_b16 v[136:137], v201 offset:4096\n"
    "v_mfma_f32_16x16x32_bf16 v[52:55], v[188:191], v[152:155], v[52:55]\n"
    "ds_read_b64_tr_b16 v[138:139], v201 offset:4608\n"
    "v_mfma_f32_16x16x32_bf16 v[60:63], v[188:191], v[156:159], v[60:63]\n"
    "ds_read_b64_tr_b16 v[140:141], v202 offset:4096\n"
    "v_mfma_f32_16x16x32_bf16 v[56:59], v[184:187], v[156:159], v[56:59]\n"
    "ds_read_b64_tr_b16 v[142:143], v202 offset:4608\n"
    "v_mfma_f32_16x16x32_bf16 v[12:15], v[84:87], v[196:199], v[12:15]\n"
    "v_mfma_f32_16x16x32_bf16 v[4:7], v[84:87], v[192:195], v[4:7]\n"
    "s_waitcnt lgkmcnt(4)\n"
    "v_mfma_f32_16x16x32_bf16 v[32:35], v[160:163], v[128:131], 0\n"
    "ds_read_b64_tr_b16 v[144:145], v201 offset:8192\n"
    "v_mfma_f32_16x16x32_bf16 v[36:39], v[164:167], v[128:131], 0\n"
    "ds_read_b64_tr_b16 v[146:147], v201 offset:8704\n"
    "v_mfma_f32_16x16x32_bf16 v[44:47], v[164:167], v[132:135], 0\n"
    "ds_read_b64_tr_b16 v[148:149], v202 offset:8192\n"
    "v_cvt_pk_bf16_f32 v72, v48, v49\n"
    "v_mfma_f32_16x16x32_bf16 v[40:43], v[160:163], v[132:135], 0\n"
    "ds_read_b64_tr_b16 v[150:151], v202 offset:8704\n"
    "v_cvt_pk_bf16_f32 v73, v50, v51\n"
    "s_waitcnt lgkmcnt(4)\n"
    "v_mfma_f32_16x16x32_bf16 v[32:35], v[168:171], v[136:139], v[32:35]\n"
    "buffer_store_dwordx4 v[0:3], v217, s[28:31], 0 offen offset:0 sc1\n"
    "ds_read_b64_tr_b16 v[152:153], v201 offset:12288\n"
    "v_mfma_f32_16x16x32_bf16 v[36:39], v[172:175], v[136:139], v[36:39]\n"
    "v_cvt_pk_bf16_f32 v74, v52, v53\n"
    "ds_read_b64_tr_b16 v[154:155], v201 offset:12800\n"
    "v_mfma_f32_16x16x32_bf16 v[44:47], v[172:175], v[140:143], v[44:47]\n"
    "v_cvt_pk_bf16_f32 v75, v54, v55\n"
    "buffer_store_dwordx4 v[4:7], v217, s[28:31], 0 offen offset:1024 sc1\n"
    "v_mfma_f32_16x16x32_bf16 v[40:43], v[168:171], v[140:143], v[40:43]\n"
    "ds_read_b64_tr_b16 v[156:157], v202 offset:12288\n"
    "v_cvt_pk_bf16_f32 v78, v60, v61\n"
    "s_waitcnt lgkmcnt(3)\n"
    "v_mfma_f32_16x16x32_bf16 v[32:35], v[176:179], v[144:147], v[32:35]\n"
    "ds_read_b64_tr_b16 v[158:159], v202 offset:12800\n"
    "v_cvt_pk_bf16_f32 v79, v62, v63\n"
    "v_mfma_f32_16x16x32_bf16 v[36:39], v[180:183], v[144:147], v[36:39]\n"
    "buffer_store_dwordx4 v[8:11], v217, s[28:31], 0 offen offset:2048 sc1\n"
    "ds_read_b64_tr_b16 v[128:129], v201 offset:16384\n"
    "v_mfma_f32_16x16x32_bf16 v[44:47], v[180:183], v[148:151], v[44:47]\n"
    "v_cvt_pk_bf16_f32 v76, v56, v57\n"
    "ds_read_b64_tr_b16 v[130:131], v201 offset:16896\n"
    "v_mfma_f32_16x16x32_bf16 v[40:43], v[176:179], v[148:151], v[40:43]\n"
    "v_cvt_pk_bf16_f32 v77, v58, v59\n"
    "buffer_store_dwordx4 v[12:15], v217, s[28:31], 0 offen offset:3072 sc1\n"
    "s_waitcnt lgkmcnt(2)\n"
    "v_mfma_f32_16x16x32_bf16 v[32:35], v[184:187], v[152:155], v[32:35]\n"
    "ds_read_b64_tr_b16 v[132:133], v202 offset:16384\n"
    "ds_read_b64_tr_b16 v[134:135], v202 offset:16896\n"
    "v_mfma_f32_16x16x32_bf16 v[36:39], v[188:191], v[152:155], v[36:39]\n"
    "ds_read_b64_tr_b16 v[136:137], v201 offset:20480\n"
    "ds_read_b64_tr_b16 v[138:139], v201 offset:20992\n"
    "v_mfma_f32_16x16x32_bf16 v[44:47], v[188:191], v[156:159], v[44:47]\n"
    "ds_read_b64_tr_b16 v[140:141], v202 offset:20480\n"
    "v_mfma_f32_16x16x32_bf16 v[40:43], v[184:187], v[156:159], v[40:43]\n"
    "ds_read_b64_tr_b16 v[142:143], v202 offset:20992\n"
    "s_waitcnt lgkmcnt(4)\n"
    "v_mfma_f32_16x16x32_bf16 v[48:51], v[160:163], v[128:131], 0\n"
    "ds_read_b64_tr_b16 v[144:145], v201 offset:24576\n"
    "v_mfma_f32_16x16x32_bf16 v[52:55], v[164:167], v[128:131], 0\n"
    "ds_read_b64_tr_b16 v[146:147], v201 offset:25088\n"
    "v_mfma_f32_16x16x32_bf16 v[60:63], v[164:167], v[132:135], 0\n"
    "ds_read_b64_tr_b16 v[148:149], v202 offset:24576\n"
    "v_cvt_pk_bf16_f32 v80, v32, v33\n"
    "v_mfma_f32_16x16x32_bf16 v[56:59], v[160:163], v[132:135], 0\n"
    "ds_read_b64_tr_b16 v[150:151], v202 offset:25088\n"
    "v_cvt_pk_bf16_f32 v81, v34, v35\n"
    "v_mfma_f32_16x16x32_bf16 v[16:19], v[72:75], v[88:91], v[16:19]\n"
    "s_waitcnt lgkmcnt(4)\n"
    "v_mfma_f32_16x16x32_bf16 v[48:51], v[168:171], v[136:139], v[48:51]\n"
    "ds_read_b64_tr_b16 v[152:153], v201 offset:28672\n"
    "v_cvt_pk_bf16_f32 v82, v36, v37\n"
    "v_mfma_f32_16x16x32_bf16 v[52:55], v[172:175], v[136:139], v[52:55]\n"
    "ds_read_b64_tr_b16 v[154:155], v201 offset:29184\n"
    "v_cvt_pk_bf16_f32 v83, v38, v39\n"
    "v_mfma_f32_16x16x32_bf16 v[60:63], v[172:175], v[140:143], v[60:63]\n"
    "ds_read_b64_tr_b16 v[156:157], v202 offset:28672\n"
    "v_mfma_f32_16x16x32_bf16 v[56:59], v[168:171], v[140:143], v[56:59]\n"
    "ds_read_b64_tr_b16 v[158:159], v202 offset:29184\n"
    "v_cvt_pk_bf16_f32 v86, v44, v45\n"
    "v_mfma_f32_16x16x32_bf16 v[24:27], v[72:75], v[92:95], v[24:27]\n"
    "v_cvt_pk_bf16_f32 v87, v46, v47\n"
    "s_waitcnt lgkmcnt(4)\n"
    "v_mfma_f32_16x16x32_bf16 v[48:51], v[176:179], v[144:147], v[48:51]\n"
    "ds_read_b64_tr_b16 v[128:129], v201 offset:32768\n"
    "v_mfma_f32_16x16x32_bf16 v[52:55], v[180:183], v[144:147], v[52:55]\n"
    "ds_read_b64_tr_b16 v[130:131], v201 offset:33280\n"
    "v_cvt_pk_bf16_f32 v84, v40, v41\n"
    "v_mfma_f32_16x16x32_bf16 v[60:63], v[180:183], v[148:151], v[60:63]\n"
    "ds_read_b64_tr_b16 v[132:133], v202 offset:32768\n"
    "v_cvt_pk_bf16_f32 v85, v42, v43\n"
    "v_mfma_f32_16x16x32_bf16 v[56:59], v[176:179], v[148:151], v[56:59]\n"
    "ds_read_b64_tr_b16 v[134:135], v202 offset:33280\n"
    "v_mfma_f32_16x16x32_bf16 v[28:31], v[76:79], v[92:95], v[28:31]\n"
    "s_waitcnt lgkmcnt(4)\n"
    "v_mfma_f32_16x16x32_bf16 v[48:51], v[184:187], v[152:155], v[48:51]\n"
    "ds_read_b64_tr_b16 v[136:137], v201 offset:36864\n"
    "v_mfma_f32_16x16x32_bf16 v[52:55], v[188:191], v[152:155], v[52:55]\n"
    "ds_read_b64_tr_b16 v[138:139], v201 offset:37376\n"
    "v_mfma_f32_16x16x32_bf16 v[60:63], v[188:191], v[156:159], v[60:63]\n"
    "ds_read_b64_tr_b16 v[140:141], v202 offset:36864\n"
    "v_mfma_f32_16x16x32_bf16 v[56:59], v[184:187], v[156:159], v[56:59]\n"
    "ds_read_b64_tr_b16 v[142:143], v202 offset:37376\n"
    "v_mfma_f32_16x16x32_bf16 v[20:23], v[76:79], v[88:91], v[20:23]\n"
    "s_waitcnt lgkmcnt(4)\n"
    "v_mfma_f32_16x16x32_bf16 v[32:35], v[160:163], v[128:131], 0\n"
    "ds_read_b64_tr_b16 v[144:145], v201 offset:40960\n"
    "v_mfma_f32_16x16x32_bf16 v[36:39], v[164:167], v[128:131], 0\n"
    "ds_read_b64_tr_b16 v[146:147], v201 offset:41472\n"
    "v_mfma_f32_16x16x32_bf16 v[44:47], v[164:167], v[132:135], 0\n"
    "ds_read_b64_tr_b16 v[148:149], v202 offset:40960\n"
    "v_cvt_pk_bf16_f32 v72, v48, v49\n"
    "v_mfma_f32_16x16x32_bf16 v[40:43], v[160:163], v[132:135], 0\n"
    "ds_read_b64_tr_b16 v[150:151], v202 offset:41472\n"
    "v_cvt_pk_bf16_f32 v73, v50, v51\n"
    "v_mfma_f32_16x16x32_bf16 v[16:19], v[80:83], v[96:99], v[16:19]\n"
    "s_waitcnt lgkmcnt(4)\n"
    "v_mfma_f32_16x16x32_bf16 v[32:35], v[168:171], v[136:139], v[32:35]\n"
    "ds_read_b64_tr_b16 v[152:153], v201 offset:45056\n"
    "v_cvt_pk_bf16_f32 v74, v52, v53\n"
    "v_mfma_f32_16x16x32_bf16 v[36:39], v[172:175], v[136:139], v[36:39]\n"
    "ds_read_b64_tr_b16 v[154:155], v201 offset:45568\n"
    "v_cvt_pk_bf16_f32 v75, v54, v55\n"
    "v_mfma_f32_16x16x32_bf16 v[44:47], v[172:175], v[140:143], v[44:47]\n"
    "ds_read_b64_tr_b16 v[156:157], v202 offset:45056\n"
    "v_mfma_f32_16x16x32_bf16 v[40:43], v[168:171], v[140:143], v[40:43]\n"
    "ds_read_b64_tr_b16 v[158:159], v202 offset:45568\n"
    "v_cvt_pk_bf16_f32 v78, v60, v61\n"
    "v_mfma_f32_16x16x32_bf16 v[24:27], v[80:83], v[100:103], v[24:27]\n"
    "v_cvt_pk_bf16_f32 v79, v62, v63\n"
    "s_waitcnt lgkmcnt(4)\n"
    "v_mfma_f32_16x16x32_bf16 v[32:35], v[176:179], v[144:147], v[32:35]\n"
    "s_waitcnt vmcnt(0)\n"
    "buffer_store_dwordx2 v[214:215], v218, s[28:31], 0 offen sc1\n"
    "ds_read_b64_tr_b16 v[128:129], v205 offset:0\n"
    "v_mfma_f32_16x16x32_bf16 v[36:39], v[180:183], v[144:147], v[36:39]\n"
    "ds_read_b64_tr_b16 v[130:131], v205 offset:512\n"
    "v_cvt_pk_bf16_f32 v76, v56, v57\n"
    "v_mfma_f32_16x16x32_bf16 v[44:47], v[180:183], v[148:151], v[44:47]\n"
    "ds_read_b64_tr_b16 v[132:133], v206 offset:0\n"
    "v_cvt_pk_bf16_f32 v77, v58, v59\n"
    "v_mfma_f32_16x16x32_bf16 v[40:43], v[176:179], v[148:151], v[40:43]\n"
    "ds_read_b64_tr_b16 v[134:135], v206 offset:512\n"
    "v_mfma_f32_16x16x32_bf16 v[28:31], v[84:87], v[100:103], v[28:31]\n"
    "s_waitcnt lgkmcnt(4)\n"
    "v_mfma_f32_16x16x32_bf16 v[32:35], v[184:187], v[152:155], v[32:35]\n"
    "ds_read_b64_tr_b16 v[136:137], v205 offset:4096\n"
    "v_mfma_f32_16x16x32_bf16 v[36:39], v[188:191], v[152:155], v[36:39]\n"
    "ds_read_b64_tr_b16 v[138:139], v205 offset:4608\n"
    "v_mfma_f32_16x16x32_bf16 v[44:47], v[188:191], v[156:159], v[44:47]\n"
    "ds_read_b64_tr_b16 v[140:141], v206 offset:4096\n"
    "v_mfma_f32_16x16x32_bf16 v[40:43], v[184:187], v[156:159], v[40:43]\n"
    "ds_read_b64_tr_b16 v[142:143], v206 offset:4608\n"
    "v_mfma_f32_16x16x32_bf16 v[20:23], v[84:87], v[96:99], v[20:23]\n"
    "s_waitcnt lgkmcnt(4)\n"
    "v_mfma_f32_16x16x32_bf16 v[48:51], v[160:163], v[128:131], 0\n"
    "ds_read_b64_tr_b16 v[144:145], v205 offset:8192\n"
    "v_mfma_f32_16x16x32_bf16 v[52:55], v[164:167], v[128:131], 0\n"
    "ds_read_b64_tr_b16 v[146:147], v205 offset:8704\n"
    "v_mfma_f32_16x16x32_bf16 v[60:63], v[164:167], v[132:135], 0\n"
    "ds_read_b64_tr_b16 v[148:149], v206 offset:8192\n"
    "v_cvt_pk_bf16_f32 v80, v32, v33\n"
    "v_mfma_f32_16x16x32_bf16 v[56:59], v[160:163], v[132:135], 0\n"
    "ds_read_b64_tr_b16 v[150:151], v206 offset:8704\n"
    "v_cvt_pk_bf16_f32 v81, v34, v35\n"
    "v_mfma_f32_16x16x32_bf16 v[16:19], v[72:75], v[64:67], v[16:19]\n"
    "s_waitcnt lgkmcnt(4)\n"
    "v_mfma_f32_16x16x32_bf16 v[48:51], v[168:171], v[136:139], v[48:51]\n"
    "ds_read_b64_tr_b16 v[152:153], v205 offset:12288\n"
    "v_cvt_pk_bf16_f32 v82, v36, v37\n"
    "v_mfma_f32_16x16x32_bf16 v[52:55], v[172:175], v[136:139], v[52:55]\n"
    "ds_read_b64_tr_b16 v[154:155], v205 offset:12800\n"
    "v_cvt_pk_bf16_f32 v83, v38, v39\n"
    "v_mfma_f32_16x16x32_bf16 v[60:63], v[172:175], v[140:143], v[60:63]\n"
    "ds_read_b64_tr_b16 v[156:157], v206 offset:12288\n"
    "v_mfma_f32_16x16x32_bf16 v[56:59], v[168:171], v[140:143], v[56:59]\n"
    "ds_read_b64_tr_b16 v[158:159], v206 offset:12800\n"
    "v_cvt_pk_bf16_f32 v86, v44, v45\n"
    "v_mfma_f32_16x16x32_bf16 v[24:27], v[72:75], v[68:71], v[24:27]\n"
    "v_cvt_pk_bf16_f32 v87, v46, v47\n"
    "s_waitcnt lgkmcnt(4)\n"
    "v_mfma_f32_16x16x32_bf16 v[48:51], v[176:179], v[144:147], v[48:51]\n"
    "ds_read_b64_tr_b16 v[128:129], v205 offset:16384\n"
    "v_mfma_f32_16x16x32_bf16 v[52:55], v[180:183], v[144:147], v[52:55]\n"
    "ds_read_b64_tr_b16 v[130:131], v205 offset:16896\n"
    "v_cvt_pk_bf16_f32 v84, v40, v41\n"
    "v_mfma_f32_16x16x32_bf16 v[60:63], v[180:183], v[148:151], v[60:63]\n"
    "ds_read_b64_tr_b16 v[132:133], v206 offset:16384\n"
    "v_cvt_pk_bf16_f32 v85, v42, v43\n"
    "v_mfma_f32_16x16x32_bf16 v[56:59], v[176:179], v[148:151], v[56:59]\n"
    "ds_read_b64_tr_b16 v[134:135], v206 offset:16896\n"
    "v_mfma_f32_16x16x32_bf16 v[28:31], v[76:79], v[68:71], v[28:31]\n"
    "s_waitcnt lgkmcnt(4)\n"
    "v_mfma_f32_16x16x32_bf16 v[48:51], v[184:187], v[152:155], v[48:51]\n"
    "ds_read_b64_tr_b16 v[136:137], v205 offset:20480\n"
    "v_mfma_f32_16x16x32_bf16 v[52:55], v[188:191], v[152:155], v[52:55]\n"
    "ds_read_b64_tr_b16 v[138:139], v205 offset:20992\n"
    "v_mfma_f32_16x16x32_bf16 v[60:63], v[188:191], v[156:159], v[60:63]\n"
    "ds_read_b64_tr_b16 v[140:141], v206 offset:20480\n"
    "v_mfma_f32_16x16x32_bf16 v[56:59], v[184:187], v[156:159], v[56:59]\n"
    "ds_read_b64_tr_b16 v[142:143], v206 offset:20992\n"
    "v_mfma_f32_16x16x32_bf16 v[20:23], v[76:79], v[64:67], v[20:23]\n"
    "s_waitcnt lgkmcnt(4)\n"
    "v_mfma_f32_16x16x32_bf16 v[32:35], v[160:163], v[128:131], 0\n"
    "ds_read_b64_tr_b16 v[144:145], v205 offset:24576\n"
    "v_mfma_f32_16x16x32_bf16 v[36:39], v[164:167], v[128:131], 0\n"
    "ds_read_b64_tr_b16 v[146:147], v205 offset:25088\n"
    "buffer_load_dwordx2 v[254:255], off, s[28:31], s70 sc1\n"
    "v_mfma_f32_16x16x32_bf16 v[44:47], v[164:167], v[132:135], 0\n"
    "ds_read_b64_tr_b16 v[148:149], v206 offset:24576\n"
    "v_cvt_pk_bf16_f32 v64, v48, v49\n"
    "v_mfma_f32_16x16x32_bf16 v[40:43], v[160:163], v[132:135], 0\n"
    "ds_read_b64_tr_b16 v[150:151], v206 offset:25088\n"
    "v_cvt_pk_bf16_f32 v65, v50, v51\n"
    "v_mfma_f32_16x16x32_bf16 v[16:19], v[80:83], v[104:107], v[16:19]\n"
    "s_waitcnt lgkmcnt(4)\n"
    "v_mfma_f32_16x16x32_bf16 v[32:35], v[168:171], v[136:139], v[32:35]\n"
    "ds_read_b64_tr_b16 v[152:153], v205 offset:28672\n"
    "v_cvt_pk_bf16_f32 v66, v52, v53\n"
    "v_mfma_f32_16x16x32_bf16 v[36:39], v[172:175], v[136:139], v[36:39]\n"
    "ds_read_b64_tr_b16 v[154:155], v205 offset:29184\n"
    "v_cvt_pk_bf16_f32 v67, v54, v55\n"
    "v_mfma_f32_16x16x32_bf16 v[44:47], v[172:175], v[140:143], v[44:47]\n"
    "ds_read_b64_tr_b16 v[156:157], v206 offset:28672\n"
    "v_mfma_f32_16x16x32_bf16 v[40:43], v[168:171], v[140:143], v[40:43]\n"
    "ds_read_b64_tr_b16 v[158:159], v206 offset:29184\n"
    "v_cvt_pk_bf16_f32 v70, v60, v61\n"
    "v_mfma_f32_16x16x32_bf16 v[24:27], v[80:83], v[108:111], v[24:27]\n"
    "v_cvt_pk_bf16_f32 v71, v62, v63\n"
    "s_waitcnt lgkmcnt(4)\n"
    "v_mfma_f32_16x16x32_bf16 v[32:35], v[176:179], v[144:147], v[32:35]\n"
    "ds_read_b64_tr_b16 v[128:129], v205 offset:32768\n"
    "v_mfma_f32_16x16x32_bf16 v[36:39], v[180:183], v[144:147], v[36:39]\n"
    "ds_read_b64_tr_b16 v[130:131], v205 offset:33280\n"
    "v_cvt_pk_bf16_f32 v68, v56, v57\n"
    "v_mfma_f32_16x16x32_bf16 v[44:47], v[180:183], v[148:151], v[44:47]\n"
    "ds_read_b64_tr_b16 v[132:133], v206 offset:32768\n"
    "v_cvt_pk_bf16_f32 v69, v58, v59\n"
    "v_mfma_f32_16x16x32_bf16 v[40:43], v[176:179], v[148:151], v[40:43]\n"
    "ds_read_b64_tr_b16 v[134:135], v206 offset:33280\n"
    "v_mfma_f32_16x16x32_bf16 v[28:31], v[84:87], v[108:111], v[28:31]\n"
    "s_waitcnt lgkmcnt(4)\n"
    "v_mfma_f32_16x16x32_bf16 v[32:35], v[184:187], v[152:155], v[32:35]\n"
    "ds_read_b64_tr_b16 v[136:137], v205 offset:36864\n"
    "v_mfma_f32_16x16x32_bf16 v[36:39], v[188:191], v[152:155], v[36:39]\n"
    "ds_read_b64_tr_b16 v[138:139], v205 offset:37376\n"
    "v_mfma_f32_16x16x32_bf16 v[44:47], v[188:191], v[156:159], v[44:47]\n"
    "ds_read_b64_tr_b16 v[140:141], v206 offset:36864\n"
    "v_mfma_f32_16x16x32_bf16 v[40:43], v[184:187], v[156:159], v[40:43]\n"
    "ds_read_b64_tr_b16 v[142:143], v206 offset:37376\n"
    "v_mfma_f32_16x16x32_bf16 v[20:23], v[84:87], v[104:107], v[20:23]\n"
    "s_waitcnt lgkmcnt(4)\n"
    "v_mfma_f32_16x16x32_bf16 v[48:51], v[160:163], v[128:131], 0\n"
    "ds_read_b64_tr_b16 v[144:145], v205 offset:40960\n"
    "v_mfma_f32_16x16x32_bf16 v[52:55], v[164:167], v[128:131], 0\n"
    "s_waitcnt vmcnt(0)\n"
    "v_cmp_eq_u64 vcc, v[254:255], v[214:215]\n"
    "s_nop 4\n"
    "s_cmp_eq_u64 vcc, -1\n"
    "s_cbranch_scc1 7f\n"
    "s_mov_b32 s67, 0x40000\n"
    "6:\n"
    "s_sleep 1\n"
    "buffer_load_dwordx2 v[254:255], off, s[28:31], s70 sc1\n"
    "s_waitcnt vmcnt(0)\n"
    "v_cmp_eq_u64 vcc, v[254:255], v[214:215]\n"
    "s_sub_u32 s67, s67, 1\n"
    "s_nop 4\n"
    "s_cmp_eq_u64 vcc, -1\n"
    "s_cbranch_scc1 7f\n"
    "s_cmp_lg_u32 s67, 0\n"
    "s_cbranch_scc1 6b\n"
    "7:\n"
    "buffer_load_dwordx4 v[80:83], v216, s[28:31], 0 offen offset:0 sc1\n"
    "buffer_load_dwordx4 v[84:87], v216, s[28:31], 0 offen offset:1024 sc1\n"
    "buffer_load_dwordx4 v[88:91], v216, s[28:31], 0 offen offset:2048 sc1\n"
    "buffer_load_dwordx4 v[92:95], v216, s[28:31], 0 offen offset:3072 sc1\n"
    "buffer_store_dwordx2 v[214:215], v208, s[28:31], 0 offen sc1\n"
    "ds_read_b64_tr_b16 v[146:147], v205 offset:41472\n"
    "v_mfma_f32_16x16x32_bf16 v[60:63], v[164:167], v[132:135], 0\n"
    "ds_read_b64_tr_b16 v[148:149], v206 offset:40960\n"
    "v_cvt_pk_bf16_f32 v72, v32, v33\n"
    "v_mfma_f32_16x16x32_bf16 v[56:59], v[160:163], v[132:135], 0\n"
    "ds_read_b64_tr_b16 v[150:151], v206 offset:41472\n"
    "v_cvt_pk_bf16_f32 v73, v34, v35\n"
    "v_mfma_f32_16x16x32_bf16 v[16:19], v[64:67], v[112:115], v[16:19]\n"
    "s_waitcnt lgkmcnt(4)\n"
    "v_mfma_f32_16x16x32_bf16 v[48:51], v[168:171], v[136:139], v[48:51]\n"
    "ds_read_b64_tr_b16 v[152:153], v205 offset:45056\n"
    "v_cvt_pk_bf16_f32 v74, v36, v37\n"
    "v_mfma_f32_16x16x32_bf16 v[52:55], v[172:175], v[136:139], v[52:55]\n"
    "ds_read_b64_tr_b16 v[154:155], v205 offset:45568\n"
    "v_cvt_pk_bf16_f32 v75, v38, v39\n"
    "v_mfma_f32_16x16x32_bf16 v[60:63], v[172:175], v[140:143], v[60:63]\n"
    "ds_read_b64_tr_b16 v[156:157], v206 offset:45056\n"
    "v_mfma_f32_16x16x32_bf16 v[56:59], v[168:171], v[140:143], v[56:59]\n"
    "ds_read_b64_tr_b16 v[158:159], v206 offset:45568\n"
    "v_cvt_pk_bf16_f32 v78, v44, v45\n"
    "v_mfma_f32_16x16x32_bf16 v[24:27], v[64:67], v[116:119], v[24:27]\n"
    "v_cvt_pk_bf16_f32 v79, v46, v47\n"
    "s_waitcnt lgkmcnt(4)\n"
    "v_mfma_f32_16x16x32_bf16 v[48:51], v[176:179], v[144:147], v[48:51]\n"
    "v_mfma_f32_16x16x32_bf16 v[52:55], v[180:183], v[144:147], v[52:55]\n"
    "v_cvt_pk_bf16_f32 v76, v40, v41\n"
    "v_mfma_f32_16x16x32_bf16 v[60:63], v[180:183], v[148:151], v[60:63]\n"
    "v_cvt_pk_bf16_f32 v77, v42, v43\n"
    "v_mfma_f32_16x16x32_bf16 v[56:59], v[176:179], v[148:151], v[56:59]\n"
    "v_mfma_f32_16x16x32_bf16 v[28:31], v[68:71], v[116:119], v[28:31]\n"
    "s_waitcnt lgkmcnt(0)\n"
    "v_mfma_f32_16x16x32_bf16 v[48:51], v[184:187], v[152:155], v[48:51]\n"
    "v_mfma_f32_16x16x32_bf16 v[52:55], v[188:191], v[152:155], v[52:55]\n"
    "v_mfma_f32_16x16x32_bf16 v[60:63], v[188:191], v[156:159], v[60:63]\n"
    "v_mfma_f32_16x16x32_bf16 v[56:59], v[184:187], v[156:159], v[56:59]\n"
    "v_mfma_f32_16x16x32_bf16 v[20:23], v[68:71], v[112:115], v[20:23]\n"
    "v_mfma_f32_16x16x32_bf16 v[16:19], v[72:75], v[120:123], v[16:19]\n"
    "s_nop 2\n"
    "v_cvt_pk_bf16_f32 v64, v48, v49\n"
    "v_cvt_pk_bf16_f32 v65, v50, v51\n"
    "v_mfma_f32_16x16x32_bf16 v[24:27], v[72:75], v[124:127], v[24:27]\n"
    "v_cvt_pk_bf16_f32 v66, v52, v53\n"
    "v_cvt_pk_bf16_f32 v67, v54, v55\n"
    "v_mfma_f32_16x16x32_bf16 v[28:31], v[76:79], v[124:127], v[28:31]\n"
    "v_cvt_pk_bf16_f32 v70, v60, v61\n"
    "v_cvt_pk_bf16_f32 v71, v62, v63\n"
    "v_mfma_f32_16x16x32_bf16 v[20:23], v[76:79], v[120:123], v[20:23]\n"
    "v_cvt_pk_bf16_f32 v68, v56, v57\n"
    "v_cvt_pk_bf16_f32 v69, v58, v59\n"
    "v_mfma_f32_16x16x32_bf16 v[16:19], v[64:67], v[192:195], v[16:19]\n"
    "v_mfma_f32_16x16x32_bf16 v[24:27], v[64:67], v[196:199], v[24:27]\n"
    "v_mfma_f32_16x16x32_bf16 v[28:31], v[68:71], v[196:199], v[28:31]\n"
    "v_mfma_f32_16x16x32_bf16 v[20:23], v[68:71], v[192:195], v[20:23]\n"
    "s_waitcnt vmcnt(1)\n"
    "s_nop 3\n"
    "v_pk_add_f32 v[16:17], v[16:17], v[80:81]\n"
    "v_pk_add_f32 v[18:19], v[18:19], v[82:83]\n"
    "v_pk_add_f32 v[24:25], v[24:25], v[88:89]\n"
    "v_pk_add_f32 v[26:27], v[26:27], v[90:91]\n"
    "v_pk_add_f32 v[28:29], v[28:29], v[92:93]\n"
    "v_pk_add_f32 v[30:31], v[30:31], v[94:95]\n"
    "v_pk_add_f32 v[20:21], v[20:21], v[84:85]\n"
    "v_pk_add_f32 v[22:23], v[22:23], v[86:87]\n"
    "v_cvt_pk_bf16_f32 v32, v16, v17\n"
    "v_cvt_pk_bf16_f32 v33, v18, v19\n"
    "v_cvt_pk_bf16_f32 v34, v20, v21\n"
    "v_cvt_pk_bf16_f32 v35, v22, v23\n"
    "v_cvt_pk_bf16_f32 v36, v24, v25\n"
    "v_cvt_pk_bf16_f32 v37, v26, v27\n"
    "v_cvt_pk_bf16_f32 v38, v28, v29\n"
    "v_cvt_pk_bf16_f32 v39, v30, v31\n"
    "v_permlane16_swap_b32 v32, v34\n"
    "v_permlane16_swap_b32 v33, v35\n"
    "buffer_store_dwordx4 v[32:35], v213, s[56:59], 0 offen\n"
    "v_permlane16_swap_b32 v36, v38\n"
    "v_permlane16_swap_b32 v37, v39\n"
    "buffer_store_dwordx4 v[36:39], v213, s[56:59], s68 offen\n"
    "s_endpgm\n"
    "91:\n"
    : "+{v200}"(aw0), "+{v201}"(aw1), "+{v202}"(aw2), "+{v203}"(aw3), "+{v204}"(ax0), "+{v205}"(ax1)
    : "{v206}"(kb), "{v207}"(sxw), "{v208}"(pst), "{v209}"(pfl), "{v210}"(wob), "{v211}"(vb), "{v212}"(prd), "{v213}"(oo), "{v216}"(xld), "{v217}"(xst), "{v218}"(xfl), "{s[68:71]}"(sc), "{s95}"(j32 * 128 + (wid ? 0x100000 : 0)),
      "{s[28:29]}"(pws), "{s[56:57]}"(pout), "{s60}"(eoff), "{s61}"(wd),
      "{s[8:9]}"(px), "{s[12:13]}"(pq), "{s[16:17]}"(pk), "{s[20:21]}"(pv), "{s[24:25]}"(po)
    : "memory", "scc", "vcc", "s30", "s31", "s58", "s59", "s62", "s63", "s64", "s65", "s66", "s67", "s96", "s97", "s98", "s99", "v0","v1","v2","v3","v4","v5","v6","v7","v8","v9","v10","v11","v12","v13","v14","v15","v16","v17","v18","v19","v20","v21","v22","v23","v24","v25","v26","v27","v28","v29","v30","v31","v32","v33","v34","v35","v36","v37","v38","v39","v40","v41","v42","v43","v44","v45","v46","v47","v48","v49","v50","v51","v52","v53","v54","v55","v56","v57","v58","v59","v60","v61","v62","v63","v64","v65","v66","v67","v68","v69","v70","v71","v72","v73","v74","v75","v76","v77","v78","v79","v80","v81","v82","v83","v84","v85","v86","v87","v88","v89","v90","v91","v92","v93","v94","v95","v96","v97","v98","v99","v100","v101","v102","v103","v104","v105","v106","v107","v108","v109","v110","v111","v112","v113","v114","v115","v116","v117","v118","v119","v120","v121","v122","v123","v124","v125","v126","v127","v128","v129","v130","v131","v132","v133","v134","v135","v136","v137","v138","v139","v140","v141","v142","v143","v144","v145","v146","v147","v148","v149","v150","v151","v152","v153","v154","v155","v156","v157","v158","v159","v160","v161","v162","v163","v164","v165","v166","v167","v168","v169","v170","v171","v172","v173","v174","v175","v176","v177","v178","v179","v180","v181","v182","v183","v184","v185","v186","v187","v188","v189","v190","v191","v192","v193","v194","v195","v196","v197","v198","v199","v214","v215","v219","v220","v221","v222","v223","v224","v225","v226","v227","v228","v229","v230","v231","v232","v233","v234","v235","v236","v237","v238","v239","v240","v241","v242","v243","v244","v245","v246","v247","v248","v249","v250","v251","v252","v253","v254","v255");
    __builtin_unreachable();
  }
}

extern "C" void kernel_launch(void* const* d_in, const int* in_sizes, int n_in, void* d_out, int out_size, void* d_ws, size_t ws_size, hipStream_t stream) {
  (void)hipFuncSetAttribute((const void*)gqa_fused, hipFuncAttributeMaxDynamicSharedMemorySize, 163840);
  Args a;
  for (int i = 0; i < 10; ++i) a.in[i] = d_in[i];
  a.out = d_out; a.ws = d_ws;
  gqa_fused<<<256, 512, 163840, stream>>>(a);
}
```
